```python
import torch

def _softmax(x, axis=-1):
    return torch.softmax(x, dim=axis)
def _gelu(x):
    return torch.nn.functional.gelu(x, approximate="tanh")
def _silu(x):
    return torch.nn.functional.silu(x)
def _sigmoid(x):
    return torch.sigmoid(x)
def _layernorm(x, axis=-1, eps=1e-5):
    m = x.mean(dim=axis, keepdim=True)
    v = x.var(dim=axis, keepdim=True, unbiased=False)
    return (x - m) / torch.sqrt(v + eps)
def _scatter_add(tgt, idx, src, axis=0):
    return tgt.clone().index_add_(axis, idx, src)
def _segment_sum(src, seg, n):
    out = torch.zeros((n,) + src.shape[1:], dtype=src.dtype)
    return out.index_add_(0, seg, src)
def _pad(x, pw):
    flat = tuple(v for pair in reversed(pw) for v in pair)
    return torch.nn.functional.pad(x, flat)

kernel_name = 'gated_mix__mha_block__dense_activation_mish__prompt'

def setup_inputs():

    _w = (lambda a: (lambda t: t.to(torch.bfloat16) if t.is_floating_point() else t)(torch.as_tensor(a)))
    def _A_setup():
        B, S, D, H = 1, 2048, 512, 32
        return {
            "x": _w((lambda *s: torch.randn(*s, dtype=torch.bfloat16) * 0.05)(B, S, D)),
            "wqkv": _w((lambda *s: torch.randn(*s, dtype=torch.bfloat16) * 0.8)(D, 3 * D)),
            "wo": _w((lambda *s: torch.randn(*s, dtype=torch.bfloat16) * 0.05)(D, D)),
        }
    _A_in = _A_setup()

    def _B_setup():
        B, S, D, F = 1, 2048, 512, 1792
        return {
            "x": _w((lambda *s: torch.randn(*s, dtype=torch.bfloat16) * 0.05)(B, S, D)),
            "w1": _w((lambda *s: torch.randn(*s, dtype=torch.bfloat16) * 12.8)(D, F) * (D ** -0.5)),
            "b1": _w((lambda *s: torch.randn(*s, dtype=torch.bfloat16) * 0.2)(F)),
            "w2": _w((lambda *s: torch.randn(*s, dtype=torch.bfloat16) * 0.1)(F, D) * (F ** -0.5)),
        }
    _B_in = _B_setup()

    return {
        "A_x": _A_in["x"],
        "A_wqkv": _A_in["wqkv"],
        "A_wo": _A_in["wo"],
        "B_x": _B_in["x"],
        "B_w1": _B_in["w1"],
        "B_b1": _B_in["b1"],
        "B_w2": _B_in["w2"],
        "gate_w": _w((lambda *s: torch.randn(*s, dtype=torch.bfloat16) * 0.05)(512) * 16.0),
    }

def reference(A_x, A_wqkv, A_wo, B_x, B_w1, B_b1, B_w2, gate_w):
    def _A_ref(x, wqkv, wo):
        B, S, D = x.shape
        H = 32
        Dh = D // H
        qkv = (lambda x, *d: x.permute(*d))((x @ wqkv).reshape(B, S, 3, H, Dh), 2, 0, 3, 1, 4)
        q, k, v = qkv[0], qkv[1], qkv[2]
        scores = torch.einsum("bhqd,bhkd->bhqk", q, k) * (Dh ** -0.5)
        p = _softmax(scores, axis=-1)
        o = torch.einsum("bhqk,bhkd->bhqd", p, v)
        return (lambda x, *d: x.permute(*d))(o, 0, 2, 1, 3).reshape(B, S, D) @ wo

    def _B_ref(x, w1, b1, w2):
        h = torch.einsum("bsd,df->bsf", x, w1) + b1
        sp = torch.log1p(torch.exp(-torch.abs(h))) + torch.where(h > 0, h, torch.zeros_like(h))
        act = h * torch.tanh(sp)
        return torch.einsum("bsf,fd->bsd", act, w2)

    x = A_x
    g = _sigmoid((x * gate_w).sum(-1, keepdims=True))
    a = _A_ref(x, A_wqkv, A_wo)
    b = _B_ref(x, B_w1, B_b1, B_w2)
    return g * a + (1 - g) * b
```

```cpp
#include <hip/hip_runtime.h>
#include <hip/hip_bf16.h>
#include <stdint.h>

typedef short bf16x8 __attribute__((ext_vector_type(8)));
typedef short s16x4 __attribute__((ext_vector_type(4)));
typedef float f32x16 __attribute__((ext_vector_type(16)));
typedef float f32x2 __attribute__((ext_vector_type(2)));
typedef __bf16 bf16x2_t __attribute__((ext_vector_type(2)));
typedef unsigned u32x4 __attribute__((ext_vector_type(4)));
typedef unsigned u32x2 __attribute__((ext_vector_type(2)));
#define LAS __attribute__((address_space(3)))
#define GAS __attribute__((address_space(1)))
typedef LAS const char* lds_cptr;
typedef LAS char* lds_ptr;
#define DI __device__ __forceinline__
#define MFMA(a, b, c) __builtin_amdgcn_mfma_f32_32x32x16_bf16((a), (b), (c), 0, 0, 0)
#define SBAR() __builtin_amdgcn_sched_barrier(0)
#define WAIT_BAR(N) asm volatile("s_waitcnt vmcnt(%0) lgkmcnt(0)\n\ts_barrier" ::"n"(N) : "memory")
#define LGKM_BAR() asm volatile("s_waitcnt lgkmcnt(0)\n\ts_barrier" ::: "memory")

constexpr int SEQ = 2048, DM = 512, NH = 32, FF = 1792;
constexpr size_t MB = 1024 * 1024;
constexpr size_t OFF_Q = 0, OFF_K = 2 * MB, OFF_V = 4 * MB, OFF_H = 6 * MB, OFF_W2F = 14 * MB, OFF_O = 16 * MB,
                 OFF_B = 18 * MB, OFF_G = 20 * MB;
constexpr float QSCALE = 0.25f * 1.4426950408889634f;

DI unsigned pk2(float a, float b) {
    f32x2 v = {a, b};
    return __builtin_bit_cast(unsigned, __builtin_convertvector(v, bf16x2_t));
}
DI u32x4 pk8(const f32x16& x, int s) {
    u32x4 r = {pk2(x[8 * s], x[8 * s + 1]), pk2(x[8 * s + 2], x[8 * s + 3]), pk2(x[8 * s + 4], x[8 * s + 5]), pk2(x[8 * s + 6], x[8 * s + 7])};
    return r;
}
DI float bflo(unsigned u) { return __uint_as_float(u << 16); }
DI float bfhi(unsigned u) { return __uint_as_float(u & 0xffff0000u); }
DI void glds16(const void* sbase, unsigned voff, unsigned lds_dst) {
    unsigned sv;
    asm volatile("s_mov_b32 %0, m0\n\ts_mov_b32 m0, %3\n\ts_nop 0\n\tglobal_load_lds_dwordx4 %1, %2\n\ts_mov_b32 m0, %0"
                 : "=&s"(sv) : "v"(voff), "s"(sbase), "s"(lds_dst) : "memory");
}
DI bf16x8 ldsr128(lds_cptr p) { return *(LAS const bf16x8*)p; }
DI s16x4 trd(lds_cptr p) { return __builtin_amdgcn_ds_read_tr16_b64_v4i16((LAS s16x4*)p); }
DI bf16x8 trd2(lds_cptr p, int second) {
    s16x4 lo = trd(p), hi = trd(p + second);
    return __builtin_shufflevector(lo, hi, 0, 1, 2, 3, 4, 5, 6, 7);
}
DI float mishf(float h) {
    const float e = __builtin_amdgcn_exp2f(h * 1.4426950408889634f);
    const float d = __builtin_fmaf(e, __builtin_fmaf(e, -0.5f, -1.0f), -1.0f);
    const float r = __builtin_amdgcn_rcpf(d);
    return __builtin_fmaf(h, r, h);
}

constexpr int K1_STAGE = 49152, K1_WOFF = 16384, K1_GATE = 3 * K1_STAGE, K1_LDS = K1_GATE + 1024;

template <int TYPE>
DI void k1_body(char* ldsmem, int rb, int cc, const char* x, const char* wsrc, int ldw2  , int col0,
                const char* b1, const char* w2, const char* gate, char* ws) {
    constexpr int NB = TYPE < 2 ? 4 : (TYPE == 2 ? 3 : 2);
    constexpr int NPW = TYPE < 2 ? 4 : (TYPE == 2 ? 3 : 2);
    constexpr int NLD = 2 + (TYPE < 2 ? 4 : 3);
    const int tid = threadIdx.x, lane = tid & 63, w = __builtin_amdgcn_readfirstlane(tid >> 6);
    const int wm = w & 3, wn = w >> 2, h = lane >> 5, l31 = lane & 31;
    const unsigned lds0 = (unsigned)(uintptr_t)ldsmem;
    const lds_cptr lds = (lds_cptr)ldsmem;
    const int xr0 = w * 16 + (lane >> 3);
    const unsigned vox0 = xr0 * 1024 + (((lane & 7) ^ ((xr0 >> 1) & 7)) << 4);
    const unsigned vox1 = (xr0 + 8) * 1024 + (((lane & 7) ^ (((xr0 + 8) >> 1) & 7)) << 4);
    const int wk = w * 8 + (lane >> 3), wch = (lane & 7) ^ (((wk >> 1) & 1) << 2);
    const unsigned vow = wk * ldw2 + wch * 16, vow2 = wk * 1024 + wch * 16;
    const char* xb = x + (size_t)rb * 128 * 1024;
    const char* wb = wsrc + col0 * 2;
    const int tt0 = (rb * 2 + (cc - 14)) * 7;
    auto stage = [&](int t) {
        const unsigned sb = lds0 + (t % 3) * K1_STAGE;
        glds16(xb + t * 128, vox0, sb + w * 2048);
        glds16(xb + t * 128, vox1, sb + w * 2048 + 1024);
#pragma unroll
        for (int p = 0; p < NPW; ++p) glds16(wb + (size_t)t * 64 * ldw2 + p * 128, vow, sb + K1_WOFF + p * 8192 + w * 1024);
        if (TYPE == 3) {
            const int tt = tt0 + (t < 6 ? t : 6);
            glds16(w2 + (size_t)(tt >> 3) * 65536 + (tt & 7) * 128, vow2, sb + K1_WOFF + 2 * 8192 + w * 1024);
        }
    };
    const bool dogate = TYPE == 3 && cc == 14 && wn == 0;
    if (TYPE == 3 && w == 0) glds16(gate, lane * 16, lds0 + K1_GATE);
    stage(0); stage(1); stage(2);
    const int xr = wm * 32 + l31;
    lds_cptr xa[4];
#pragma unroll
    for (int s = 0; s < 4; ++s) xa[s] = lds + xr * 128 + (((2 * s + h) ^ ((xr >> 1) & 7)) << 4);
    const int q = (lane & 15) >> 2, g = (lane >> 4) & 1, p4 = lane & 3;
    lds_cptr wa[NB];
#pragma unroll
    for (int b = 0; b < NB; ++b) {
        const int bb = wn * NB + b;
        wa[b] = lds + K1_WOFF + (bb >> 1) * 8192 + (8 * h + q) * 128 + ((((bb & 1) ^ (q >> 1)) & 1) << 6) + g * 32 + p4 * 8;
    }
    const lds_cptr w2a = lds + K1_WOFF + 2 * 8192 + (16 * (w >> 1) + 4 * h + q) * 128 + ((((w & 1) ^ (q >> 1)) & 1) << 6) + g * 32 + p4 * 8;
    f32x16 acc[NB], gacc = {};
#pragma unroll
    for (int b = 0; b < NB; ++b) acc[b] = f32x16{};
    u32x2 bias[NB][4];
#pragma unroll
    for (int t = 0; t < 8; ++t) {
        if (t == 0) WAIT_BAR(2 * NLD); else if (t < 7) WAIT_BAR(NLD); else WAIT_BAR(0);
        if (t >= 1 && t + 2 < 8) stage(t + 2);
        if (TYPE >= 2 && t == 7) {
#pragma unroll
            for (int b = 0; b < NB; ++b)
#pragma unroll
                for (int i = 0; i < 4; ++i) bias[b][i] = *(GAS const u32x2*)(b1 + (col0 + (wn * NB + b) * 32 + 8 * i + 4 * h) * 2);
        }
        const int so = (t % 3) * K1_STAGE;
#pragma unroll
        for (int s = 0; s < 4; ++s) {
            const bf16x8 xf = ldsr128(xa[s] + so);
#pragma unroll
            for (int b = 0; b < NB; ++b) {
                const bf16x8 wf = trd2(wa[b] + so + s * 2048, 512);
                acc[b] = TYPE == 1 ? MFMA(xf, wf, acc[b]) : MFMA(wf, xf, acc[b]);
            }
            if (TYPE == 3) {
                if (dogate) gacc = MFMA(ldsr128(lds + K1_GATE + (t * 64 + 16 * s + 8 * h) * 2), xf, gacc);
            }
        }
        if (TYPE == 3 && t < 7) {
            const int tt = tt0 + t;
            const bf16x8 f = trd2(w2a + so, 1024);
            *(GAS bf16x8*)(ws + OFF_W2F + ((size_t)(((tt & 7) * 2 + (w & 1)) * 112 + (tt >> 3) * 4 + (w >> 1)) * 64 + lane) * 16) = f;
        }
    }
    const int tok0 = rb * 128 + wm * 32;
    if (TYPE == 0) {
        const float sc = cc < 2 ? QSCALE : 1.0f;
        char* dst = ws + (cc < 2 ? OFF_Q : OFF_K);
#pragma unroll
        for (int b = 0; b < NB; ++b) {
            const f32x16 v = acc[b] * sc;
#pragma unroll
            for (int s = 0; s < 2; ++s) {
                const int head = (cc & 1) * 16 + (wn * NB + b) * 2 + s;
                *(GAS u32x4*)(dst + ((size_t)head * SEQ + tok0 + l31) * 32 + h * 16) = pk8(v, s);
            }
        }
    } else if (TYPE == 1) {
#pragma unroll
        for (int b = 0; b < NB; ++b)
#pragma unroll
            for (int s = 0; s < 2; ++s) {
                const int head = (cc & 1) * 16 + (wn * NB + b) * 2 + (l31 >> 4);
                *(GAS u32x4*)(ws + OFF_V + ((size_t)head * 128 + (tok0 >> 4) + s) * 512 + h * 256 + (lane & 15) * 16) = pk8(acc[b], s);
            }
    } else {
#pragma unroll
        for (int b = 0; b < NB; ++b) {
            f32x16 v;
#pragma unroll
            for (int i = 0; i < 4; ++i) {
                v[4 * i + 0] = mishf(acc[b][4 * i + 0] + bflo(bias[b][i][0]));
                v[4 * i + 1] = mishf(acc[b][4 * i + 1] + bfhi(bias[b][i][0]));
                v[4 * i + 2] = mishf(acc[b][4 * i + 2] + bflo(bias[b][i][1]));
                v[4 * i + 3] = mishf(acc[b][4 * i + 3] + bfhi(bias[b][i][1]));
            }
#pragma unroll
            for (int s = 0; s < 2; ++s) {
                const int fg = (col0 >> 4) + (wn * NB + b) * 2 + s;
                *(GAS u32x4*)(ws + OFF_H + ((size_t)((tok0 >> 5) * 112 + fg) * 64 + lane) * 16) = pk8(v, s);
            }
        }
        if (TYPE == 3) {
            if (dogate && h == 0) {
                const float e = __builtin_amdgcn_exp2f(gacc[0] * -1.4426950408889634f);
                *(GAS float*)(ws + OFF_G + (size_t)(tok0 + l31) * 4) = __builtin_amdgcn_rcpf(1.0f + e);
            }
        }
    }
}

__global__ __launch_bounds__(512, 2) void k1_proj(const char* __restrict__ x, const char* __restrict__ wqkv, const char* __restrict__ w1,
                                                  const char* __restrict__ b1, const char* __restrict__ w2, const char* __restrict__ gate,
                                                  char* __restrict__ ws) {
    __shared__ __attribute__((aligned(1024))) char lds[K1_LDS];
    const int bid = blockIdx.x, xcd = bid & 7, idx = bid >> 3;
    const int rb = (xcd & 1) * 8 + (idx & 7);
    const int cc = (int)((0xDCBA9854F732E610ull >> (4 * ((xcd >> 1) * 4 + (idx >> 3)))) & 15);
    if (cc < 4) k1_body<0>(lds, rb, cc, x, wqkv, 3072, cc * 256, b1, w2, gate, ws);
    else if (cc < 6) k1_body<1>(lds, rb, cc, x, wqkv, 3072, cc * 256, b1, w2, gate, ws);
    else if (cc < 14) k1_body<2>(lds, rb, cc, x, w1, 3584, (cc - 6) * 192, b1, w2, gate, ws);
    else k1_body<3>(lds, rb, cc, x, w1, 3584, 1536 + (cc - 14) * 128, b1, w2, gate, ws);
}

constexpr int A_K = 0, A_V = 65536, A_ONES = 131072, A_X = A_ONES + 4096, A_FLAG = A_X + 18432, A_LDS = A_FLAG + 16;

DI f32x16 exp16(const f32x16& s) {
    f32x16 r;
#pragma unroll
    for (int i = 0; i < 16; ++i) r[i] = __builtin_amdgcn_exp2f(s[i]);
    return r;
}
#define BC8(x) __builtin_bit_cast(bf16x8, (x))

__global__ __launch_bounds__(768, 3) void k2_attn(char* __restrict__ ws) {
    __shared__ __attribute__((aligned(1024))) char ldsmem[A_LDS];
    const int tid = threadIdx.x, lane = tid & 63, w = __builtin_amdgcn_readfirstlane(tid >> 6);
    const int h = lane >> 5, l31 = lane & 31;
    const int bid = blockIdx.x, xcd = bid & 7, idx = bid >> 3;
    const unsigned lds0 = (unsigned)(uintptr_t)ldsmem;
    const lds_ptr lds = (lds_ptr)ldsmem;
    if (w < 8) {
        const int head = xcd * 4 + (idx >> 3), qblk = idx & 7, half = w >> 2;
        const char* kg = ws + OFF_K + (size_t)head * 65536;
        const char* vg = ws + OFF_V + (size_t)head * 65536;
        const unsigned vo = lane * 16;
        glds16(ws + OFF_Q + ((size_t)head * SEQ + qblk * 256 + w * 32) * 32, vo, lds0 + A_X + w * 1024);
        { const int o = half * 32768 + (w & 3) * 1024; glds16(kg + o, vo, lds0 + A_K + o); glds16(vg + o, vo, lds0 + A_V + o); }
#pragma unroll
        for (int i = 1; i < 8; ++i) { const int o = half * 32768 + (i * 4 + (w & 3)) * 1024; glds16(kg + o, vo, lds0 + A_K + o); glds16(vg + o, vo, lds0 + A_V + o); }
        WAIT_BAR(14);
        const bf16x8 qA = ldsr128(lds + A_X + (w & 3) * 2048 + l31 * 32 + h * 16);
        const bf16x8 qB = ldsr128(lds + A_X + (w & 3) * 2048 + 1024 + l31 * 32 + h * 16);
        const lds_cptr kp = lds + A_K + half * 32768 + l31 * 32 + h * 16;
        const bool isone = (lane & 16) != 0;
        lds_cptr vp = lds + (isone ? A_ONES : A_V + half * 32768) + h * 256 + (lane & 15) * 16;
        const int vstep = isone ? 0 : 4096;
        f32x16 OA = {}, OB = {}, SA, SB;
        const f32x16 Z = {};
        bf16x8 kf = ldsr128(kp);
        SA = MFMA(kf, qA, Z);
#define TILE(t, u)                                                                                   \
        {                                                                                            \
            const bf16x8 v0 = ldsr128(vp + (u) * 1024), v1 = ldsr128(vp + (u) * 1024 + 512);         \
            SB = MFMA(kf, qB, Z);                                                                    \
            kf = ldsr128(kp + (((t) + 1) & 31) * 1024);                                              \
            { const f32x16 p = exp16(SA); OA = MFMA(v0, BC8(pk8(p, 0)), OA); OA = MFMA(v1, BC8(pk8(p, 1)), OA); } \
            SA = MFMA(kf, qA, Z);                                                                    \
            { const f32x16 p = exp16(SB); OB = MFMA(v0, BC8(pk8(p, 0)), OB); OB = MFMA(v1, BC8(pk8(p, 1)), OB); } \
        }
        TILE(0, 0) TILE(1, 1) TILE(2, 2)
        WAIT_BAR(0);
        TILE(3, 3)
        vp += vstep;
        for (int t = 4; t < 32; t += 4) {
            TILE(t, 0) TILE(t + 1, 1) TILE(t + 2, 2) TILE(t + 3, 3)
            vp += vstep;
        }
        LGKM_BAR();
        {
            const f32x16& give = half ? OA : OB;
            LAS float* dst = (LAS float*)(lds + A_X + w * 2304) + lane;
#pragma unroll
            for (int r = 0; r < 9; ++r) dst[r * 64] = give[r];
        }
        LGKM_BAR();
        f32x16 O = half ? OB : OA;
        {
            LAS const float* src = (LAS const float*)(lds + A_X + (w ^ 4) * 2304) + lane;
#pragma unroll
            for (int r = 0; r < 9; ++r) O[r] += src[r * 64];
        }
        bool bad = !(O[8] > 0x1p-90f && O[8] < 0x1p100f);
#pragma unroll
        for (int r = 0; r < 8; ++r) bad |= !(__builtin_fabsf(O[r]) < 0x1p120f);
        if (__any(bad)) {
            const bf16x8 qf = half ? qB : qA;
            float m = -INFINITY;
            O = Z;
            const lds_cptr kq = lds + A_K + l31 * 32 + h * 16;
            const lds_cptr vq = lds + (isone ? A_ONES : A_V) + h * 256 + (lane & 15) * 16;
            for (int t = 0; t < 64; ++t) {
                f32x16 s = MFMA(ldsr128(kq + t * 1024), qf, Z);
                float tm = s[0];
#pragma unroll
                for (int r = 1; r < 16; ++r) tm = __builtin_fmaxf(tm, s[r]);
                tm = __builtin_fmaxf(tm, __shfl_xor(tm, 32));
                const float mn = __builtin_fmaxf(m, tm);
                const float al = __builtin_amdgcn_exp2f(m - mn);
                m = mn;
#pragma unroll
                for (int r = 0; r < 16; ++r) { O[r] *= al; s[r] = __builtin_amdgcn_exp2f(s[r] - mn); }
                const int vo2 = isone ? 0 : t * 1024;
                O = MFMA(ldsr128(vq + vo2), BC8(pk8(s, 0)), O);
                O = MFMA(ldsr128(vq + vo2 + 512), BC8(pk8(s, 1)), O);
            }
        }
        const float rl = 1.0f / O[8];
        const f32x16 on = O * rl;
        const int mblk = qblk * 8 + (w & 3) * 2 + half;
        *(GAS u32x4*)(ws + OFF_O + ((size_t)(mblk * 32 + head) * 64 + lane) * 16) = pk8(on, 0);
    } else {
        const int j = w - 8;
        { const u32x4 one = {0x3f803f80u, 0x3f803f80u, 0x3f803f80u, 0x3f803f80u}; *(LAS u32x4*)(lds + A_ONES + (tid - 512) * 16) = one; }
        if (tid == 512) *(LAS volatile int*)(lds + A_FLAG) = 0;
        const int mt = xcd * 4 + (idx >> 3), nt = idx & 7;
        const GAS bf16x8* hp = (const GAS bf16x8*)(ws + OFF_H) + (size_t)(mt * 2 * 112 + j * 28) * 64 + lane;
        const GAS bf16x8* wp = (const GAS bf16x8*)(ws + OFF_W2F) + (size_t)(nt * 2 * 112 + j * 28) * 64 + lane;
        constexpr int PD = 4;
        bf16x8 hf[PD][2], wf[PD][2];
#pragma unroll
        for (int i = 0; i < PD; ++i) { hf[i][0] = hp[i * 64]; hf[i][1] = hp[(112 + i) * 64]; wf[i][0] = wp[i * 64]; wf[i][1] = wp[(112 + i) * 64]; }
        LGKM_BAR();
        asm volatile("s_barrier" ::: "memory");
        f32x16 acc[2][2] = {};
#pragma unroll
        for (int k = 0; k < 28; ++k) {
            const int i = k % PD;
#pragma unroll
            for (int n = 0; n < 2; ++n)
#pragma unroll
                for (int m = 0; m < 2; ++m) acc[n][m] = MFMA(wf[i][n], hf[i][m], acc[n][m]);
            if (k + PD < 28) { hf[i][0] = hp[(k + PD) * 64]; hf[i][1] = hp[(112 + k + PD) * 64]; wf[i][0] = wp[(k + PD) * 64]; wf[i][1] = wp[(112 + k + PD) * 64]; }
        }
        LAS volatile int* flag = (LAS volatile int*)(lds + A_FLAG);
        LAS f32x16* red = (LAS f32x16*)(lds + A_X) + lane;
        if (j < 3) {
            while (*flag != 3 - j) __builtin_amdgcn_s_sleep(4);
            asm volatile("" ::: "memory");
#pragma unroll
            for (int i = 0; i < 4; ++i) acc[i >> 1][i & 1] += red[i * 64];
        }
        if (j > 0) {
#pragma unroll
            for (int i = 0; i < 4; ++i) red[i * 64] = acc[i >> 1][i & 1];
            asm volatile("s_waitcnt lgkmcnt(0)" ::: "memory");
            if (lane == 0) *flag = 4 - j;
        } else {
#pragma unroll
            for (int i = 0; i < 4; ++i) {
                GAS u32x4* dst = (GAS u32x4*)(ws + OFF_B + ((size_t)((mt * 8 + nt) * 4 + i) * 2) * 1024) + lane;
                dst[0] = pk8(acc[i >> 1][i & 1], 0);
                dst[64] = pk8(acc[i >> 1][i & 1], 1);
            }
        }
    }
}

__global__ __launch_bounds__(256, 1) void k3_out(const char* __restrict__ wo, const char* __restrict__ ws, char* __restrict__ out) {
    __shared__ __attribute__((aligned(1024))) char ldsmem[131072];
    const int tid = threadIdx.x, lane = tid & 63, w = __builtin_amdgcn_readfirstlane(tid >> 6);
    const int h = lane >> 5, l31 = lane & 31;
    const int bid = blockIdx.x, xcd = bid & 7, idx = bid >> 3;
    const int mt = xcd * 4 + (idx >> 3), nt = idx & 7;
    const int wn = w >> 1, wm = w & 1;
    const unsigned lds0 = (unsigned)(uintptr_t)ldsmem;
    const lds_cptr lds = (lds_cptr)ldsmem;
    const GAS u32x4* bp = (const GAS u32x4*)(ws + OFF_B + ((size_t)((mt * 8 + nt) * 4 + w) * 2) * 1024) + lane;
    const u32x4 b0 = bp[0], b1 = bp[64];
    const int row = mt * 64 + wm * 32 + l31;
    const float g = *(const GAS float*)(ws + OFF_G + (size_t)row * 4);
    const char* og = ws + OFF_O + (size_t)mt * 65536;
    const char* wg = wo + nt * 128;
    const int wch = (lane & 7) ^ (((lane >> 4) & 1) << 2);
    const unsigned vow = (lane >> 3) * 1024 + wch * 16;
#pragma unroll
    for (int c = 0; c < 4; ++c) {
#pragma unroll
        for (int i = 0; i < 4; ++i) {
            const int po = (w & 1) * 32 + c * 8 + (w >> 1) * 4 + i;
            glds16(og + po * 1024, lane * 16, lds0 + po * 1024);
            const int pw = c * 16 + w * 4 + i;
            glds16(wg + (size_t)pw * 8192, vow, lds0 + 65536 + pw * 1024);
        }
    }
    const int q = (lane & 15) >> 2, gg = (lane >> 4) & 1, p4 = lane & 3;
    const lds_cptr oa = lds + wm * 32768 + lane * 16;
    const lds_cptr wa = lds + 65536 + (4 * h + q) * 128 + (((wn ^ (q >> 1)) & 1) << 6) + gg * 32 + p4 * 8;
    f32x16 acc = {};
#pragma unroll
    for (int c = 0; c < 4; ++c) {
        if (c == 0) WAIT_BAR(24); else if (c == 1) WAIT_BAR(16); else if (c == 2) WAIT_BAR(8); else WAIT_BAR(0);
#pragma unroll
        for (int i = 0; i < 8; ++i) {
            const int kg = c * 8 + i;
            acc = MFMA(trd2(wa + kg * 2048, 1024), ldsr128(oa + kg * 1024), acc);
        }
    }
    const float og1 = 1.0f - g;
    unsigned o[8];
#pragma unroll
    for (int i = 0; i < 4; ++i) {
        o[i] = pk2(g * acc[2 * i] + og1 * bflo(b0[i]), g * acc[2 * i + 1] + og1 * bfhi(b0[i]));
        o[4 + i] = pk2(g * acc[8 + 2 * i] + og1 * bflo(b1[i]), g * acc[8 + 2 * i + 1] + og1 * bfhi(b1[i]));
    }
#pragma unroll
    for (int k = 0; k < 2; ++k) {
        auto r0 = __builtin_amdgcn_permlane32_swap(o[4 * k], o[4 * k + 2], false, false);
        auto r1 = __builtin_amdgcn_permlane32_swap(o[4 * k + 1], o[4 * k + 3], false, false);
        const u32x4 v = {r0[0], r1[0], r0[1], r1[1]};
        *(GAS u32x4*)(out + (size_t)row * 1024 + (nt * 64 + wn * 32 + 16 * k + 8 * h) * 2) = v;
    }
}

extern "C" void kernel_launch(void* const* d_in, const int* in_sizes, int n_in, void* d_out, int out_size, void* d_ws, size_t ws_size,
                              hipStream_t stream) {
    const char* x = (const char*)d_in[0];
    const char* wqkv = (const char*)d_in[1];
    const char* wo = (const char*)d_in[2];
    const char* w1 = (const char*)d_in[4];
    const char* b1 = (const char*)d_in[5];
    const char* w2 = (const char*)d_in[6];
    const char* gate = (const char*)d_in[7];
    char* ws = (char*)d_ws;
    k1_proj<<<256, 512, 0, stream>>>(x, wqkv, w1, b1, w2, gate, ws);
    k2_attn<<<256, 768, 0, stream>>>(ws);
    k3_out<<<256, 256, 0, stream>>>(wo, ws, (char*)d_out);
}
```

```cpp
#include <hip/hip_runtime.h>
#include <hip/hip_bf16.h>
#include <stdint.h>

typedef short bf16x8 __attribute__((ext_vector_type(8)));
typedef short s16x4 __attribute__((ext_vector_type(4)));
typedef float f32x16 __attribute__((ext_vector_type(16)));
typedef float f32x2 __attribute__((ext_vector_type(2)));
typedef float f32x4 __attribute__((ext_vector_type(4)));
typedef __bf16 bf16x2_t __attribute__((ext_vector_type(2)));
typedef unsigned u32x4 __attribute__((ext_vector_type(4)));
typedef unsigned u32x2 __attribute__((ext_vector_type(2)));
typedef int i32x4 __attribute__((ext_vector_type(4)));
typedef unsigned long long u64;
#define LAS __attribute__((address_space(3)))
#define GAS __attribute__((address_space(1)))
typedef LAS const char* lds_cptr;
typedef LAS char* lds_ptr;
#define DI __device__ __forceinline__
#define MFMA(a, b, c) __builtin_amdgcn_mfma_f32_32x32x16_bf16((a), (b), (c), 0, 0, 0)
#define SBAR() __builtin_amdgcn_sched_barrier(0)
#define PIN(x) asm volatile("" : "+v"(x))
#define VMW(N) asm volatile("s_waitcnt vmcnt(%0)" ::"n"(N) : "memory")
#define LGKM_BAR() asm volatile("s_waitcnt lgkmcnt(0)\n\ts_barrier" ::: "memory")
#define VM_BAR(N) asm volatile("s_waitcnt vmcnt(%0)\n\ts_barrier" ::"n"(N) : "memory")
#define LGW() asm volatile("s_waitcnt lgkmcnt(0)" ::: "memory")
#define BC8(x) __builtin_bit_cast(bf16x8, (x))
#define RFL(x) __builtin_amdgcn_readfirstlane(x)

constexpr int SEQ = 2048;
constexpr unsigned MB = 1024 * 1024;
constexpr unsigned OFF_Q = 0, OFF_K = 2 * MB, OFF_V = 4 * MB, OFF_H = 6 * MB, OFF_W2F = 14 * MB, OFF_O = 16 * MB, OFF_G = 18 * MB,
                   OFF_FQ = 18 * MB + 16384, OFF_FH = OFF_FQ + 2048, OFF_FO = OFF_FH + 2048, OFF_FW = OFF_FO + 2048, OFF_EP = OFF_FW + 2048,
                   WS_USED = 19 * MB;
constexpr float LOG2E = 1.4426950408889634f;
constexpr float QSCALE = 0.25f * LOG2E;

constexpr int L_KV = 0;
constexpr int L_RING = 0, NSLOT = 8;
constexpr int L_X = 131072;
constexpr int L_W2 = 147456, L_GATE = 155648, L_ONES = 156672, L_SYNC = 160768, L_TOTAL = L_SYNC + 512;
constexpr int L_BIAS = L_SYNC + 256;
constexpr int S_QRD = 0, S_KVM = 48, S_QOK = 80, S_CNTQ = 84, S_CNTH = 88, S_DONE = 92, S_CNTO = 96, S_OOK = 100,
              S_HOK = 104, S_FX = 112  , S_FB = 144, S_WOF = 160  , S_FY = 192;

#define TRACE(kid, i)
#define STAMP_DECL
#define STAMP()
#define STAMP1()
#define STAMP_RT()
#define STAMP_OUT(kid, wsp)

DI unsigned pk2(float a, float b) {
    f32x2 v = {a, b};
    return __builtin_bit_cast(unsigned, __builtin_convertvector(v, bf16x2_t));
}
DI u32x4 pk8(const f32x16& x, int s) {
    u32x4 r = {pk2(x[8 * s], x[8 * s + 1]), pk2(x[8 * s + 2], x[8 * s + 3]), pk2(x[8 * s + 4], x[8 * s + 5]), pk2(x[8 * s + 6], x[8 * s + 7])};
    return r;
}
DI float bflo(unsigned u) { return __uint_as_float(u << 16); }
DI float bfhi(unsigned u) { return __uint_as_float(u & 0xffff0000u); }
DI void glds16(const void* sbase, unsigned voff, unsigned lds_dst) {
    unsigned sv;
    asm volatile("s_mov_b32 %0, m0\n\ts_mov_b32 m0, %3\n\ts_nop 0\n\tglobal_load_lds_dwordx4 %1, %2\n\ts_mov_b32 m0, %0"
                 : "=&s"(sv) : "v"(voff), "s"(sbase), "s"(lds_dst) : "memory");
}
DI void glds1(const void* sbase, unsigned voff, unsigned lds_dst) {
    unsigned sv;
    asm volatile("s_mov_b32 %0, m0\n\ts_mov_b32 m0, %3\n\ts_nop 0\n\tglobal_load_lds_dword %1, %2\n\ts_mov_b32 m0, %0"
                 : "=&s"(sv) : "v"(voff), "s"(sbase), "s"(lds_dst) : "memory");
}
#define GLDS4_BODY(POL)                                                                                                    \
    unsigned sv;                                                                                                           \
    asm volatile("s_mov_b32 %0, m0\n\ts_mov_b32 m0, %6\n\ts_nop 0\n\tglobal_load_lds_dwordx4 %1, %5" POL                   \
                 "\n\tglobal_load_lds_dwordx4 %2, %5 offset:1024" POL "\n\tglobal_load_lds_dwordx4 %3, %5 offset:2048" POL \
                 "\n\tglobal_load_lds_dwordx4 %4, %5 offset:3072" POL "\n\ts_mov_b32 m0, %0"                               \
                 : "=&s"(sv) : "v"(v0), "v"(v1), "v"(v2), "v"(v3), "s"(sbase), "s"(lds_dst) : "memory")
DI void glds4(const void* sbase, unsigned v0, unsigned v1, unsigned v2, unsigned v3, unsigned lds_dst) { GLDS4_BODY(""); }
DI void glds4c(const void* sbase, unsigned v0, unsigned lds_dst) { const unsigned v1 = v0, v2 = v0, v3 = v0; GLDS4_BODY(" sc1"); }
DI bf16x8 ldsr128(lds_cptr p) { return *(LAS const bf16x8*)p; }
DI s16x4 trd(lds_cptr p) { return __builtin_amdgcn_ds_read_tr16_b64_v4i16((LAS s16x4*)p); }
DI bf16x8 trd2(lds_cptr p, int second) {
    s16x4 lo = trd(p), hi = trd(p + second);
    return __builtin_shufflevector(lo, hi, 0, 1, 2, 3, 4, 5, 6, 7);
}
DI f32x16 mish16(const f32x16& h) {
    f32x16 e = h * LOG2E;
#pragma unroll
    for (int i = 0; i < 16; ++i) e[i] = __builtin_amdgcn_exp2f(e[i]);
    const f32x16 mh = {-0.5f, -0.5f, -0.5f, -0.5f, -0.5f, -0.5f, -0.5f, -0.5f, -0.5f, -0.5f, -0.5f, -0.5f, -0.5f, -0.5f, -0.5f, -0.5f};
    const f32x16 m1 = mh + mh;
    f32x16 d = __builtin_elementwise_fma(e, __builtin_elementwise_fma(e, mh, m1), m1);
#pragma unroll
    for (int i = 0; i < 16; ++i) d[i] = __builtin_amdgcn_rcpf(d[i]);
    return __builtin_elementwise_fma(h, d, h);
}
DI bool vm_idle() {
    unsigned r;
    asm volatile("s_getreg_b32 %0, hwreg(HW_REG_IB_STS, 0, 4)" : "=s"(r)::"memory");
    return r == 0;
}
DI void sig(lds_ptr sy, int off, int v) {
    asm volatile("s_mov_b64 exec, 1\n\tds_write_b32 %0, %1\n\ts_mov_b64 exec, -1" ::"v"((unsigned)(uintptr_t)(sy + off)), "v"(v) : "memory");
}
#define UNLIKELY(x) __builtin_expect(!!(x), 0)
DI void spin1(lds_ptr sy, int off, int need) {
    while (RFL(*(LAS volatile int*)(sy + off)) < need) __builtin_amdgcn_s_sleep(1);
    asm volatile("" ::: "memory");
}
DI int min4(lds_ptr sy, int off) {
    const i32x4 v = *(LAS volatile i32x4*)(sy + off);
    return RFL(min(min(v[0], v[1]), min(v[2], v[3])));
}
DI int min8(lds_ptr sy, int off) {
    const i32x4 v = *(LAS volatile i32x4*)(sy + off), u = *(LAS volatile i32x4*)(sy + off + 16);
    return RFL(min(min(min(v[0], v[1]), min(v[2], v[3])), min(min(u[0], u[1]), min(u[2], u[3]))));
}
DI void spin4(lds_ptr sy, int off, int need) {
    while (min4(sy, off) < need) __builtin_amdgcn_s_sleep(1);
    asm volatile("" ::: "memory");
}
DI void spin4f(lds_ptr sy, int off, int need) {
    while (min4(sy, off) < need) {}
    asm volatile("" ::: "memory");
}
DI int lds_inc(lds_ptr sy, int off) {
    int r = 0;
    if ((threadIdx.x & 63) == 0) r = __hip_atomic_fetch_add((LAS int*)(sy + off), 1, __ATOMIC_RELAXED, __HIP_MEMORY_SCOPE_WORKGROUP);
    return RFL(r);
}
#define POLL_SLEEP 2
DI void flag_set(char* ws, unsigned off, u64 tag) {
    if ((threadIdx.x & 63) == 0) __hip_atomic_store((GAS u64*)(ws + off), tag, __ATOMIC_RELAXED, __HIP_MEMORY_SCOPE_AGENT);
}
template <int N>
DI void flag_wait(char* ws, unsigned laneoff, u64 tag) {
    GAS u64* p = (GAS u64*)(ws + laneoff);
    for (int spins = 0; spins < (1 << 20); ++spins) {
        bool ok = true;
#pragma unroll
        for (int i = 0; i < N; ++i) ok &= __hip_atomic_load(p + i * 64, __ATOMIC_RELAXED, __HIP_MEMORY_SCOPE_AGENT) == tag;
        if (__all(ok)) break;
        __builtin_amdgcn_s_sleep(POLL_SLEEP);
    }
    asm volatile("" ::: "memory");
}
#define WS_STORE(v, off) __builtin_amdgcn_raw_buffer_store_b128((v), wsr, (off), 0, 16)
#define WS_LOAD(voff, soff) __builtin_amdgcn_raw_buffer_load_b128(wsr, (voff), (soff), 16)

struct Geo {
    int lane, w, wm, h, l31, rot, q, tcol, sw;
};

template <int NB, bool SWAP, bool GATE, class Init, class Hook>
DI void proj_pass(lds_ptr lds, const Geo& g, int woff, f32x16 (&acc)[2], Init init, Hook hook) {
    const int xr = g.wm * 32 + g.l31;
    int xo[4];
#pragma unroll
    for (int s = 0; s < 4; ++s) xo[s] = L_RING + 16384 + xr * 128 + (((2 * s + g.h) ^ ((xr >> 1) & 7)) << 4);
    const int w0 = (L_RING + (8 * g.h + g.q) * 128 + g.sw + g.tcol) ^ woff, w1 = w0 ^ 64;
    constexpr int NF = NB + (GATE ? 1 : 0);
    bf16x8 xf[4], wf[4][NF];
    auto rd = [&](int s, int b, int t) {
        const int so = (t & 3) * 32768;
        if (b == 0) xf[s] = ldsr128(lds + xo[s] + so);
        if (b < NB) wf[s][b] = trd2(lds + ((b ? w1 : w0) + so) + s * 2048, 512);
        else wf[s][b] = ldsr128(lds + L_GATE + ((t + g.rot) & 7) * 128 + g.h * 16 + s * 32);
        SBAR();
    };
    auto mm = [&](int s, int b) {
        acc[b] = SWAP ? MFMA(xf[s], wf[s][b], acc[b]) : MFMA(wf[s][b], xf[s], acc[b]);
        SBAR();
    };
    LGKM_BAR(); SBAR();
    init();
#pragma unroll
    for (int s = 0; s < 4; ++s)
#pragma unroll
        for (int b = 0; b < NF; ++b) { if (s >= 2) mm(s - 2, b); rd(s, b, 0); }
#pragma unroll 1
    for (int t = 1; t < 8; ++t) {
        hook();
        LGKM_BAR(); SBAR();
#pragma unroll
        for (int s = 0; s < 4; ++s)
#pragma unroll
            for (int b = 0; b < NF; ++b) { mm((s + 2) & 3, b); rd(s, b, t); }
    }
#pragma unroll
    for (int s = 2; s < 4; ++s)
#pragma unroll
        for (int b = 0; b < NF; ++b) mm(s, b);
}
DI f32x16 bias16(lds_cptr p) {
    f32x16 v;
#pragma unroll
    for (int i = 0; i < 4; ++i) {
        const u32x2 bb = *(LAS const u32x2*)(p + 16 * i);
        v[4 * i] = bflo(bb[0]); v[4 * i + 1] = bfhi(bb[0]); v[4 * i + 2] = bflo(bb[1]); v[4 * i + 3] = bfhi(bb[1]);
    }
    return v;
}

#define LOOKA 8
#define PACE 2

__global__ __launch_bounds__(768, 3) void fused_block(const char* __restrict__ x, const char* __restrict__ wqkv, const char* __restrict__ wo,
                                                      const char* __restrict__ w1, const char* __restrict__ b1, const char* __restrict__ w2,
                                                      const char* __restrict__ gate, char* __restrict__ ws, char* __restrict__ out) {
    __shared__ __attribute__((aligned(1024))) char ldsmem[L_TOTAL];
    const int tid = threadIdx.x, lane = tid & 63, w = RFL(tid >> 6), wm = w & 3, h = lane >> 5, l31 = lane & 31;
    const int bid = blockIdx.x, xcd = bid & 7, idx = bid >> 3;
    const unsigned lds0 = (unsigned)(uintptr_t)ldsmem;
    const lds_ptr lds = (lds_ptr)ldsmem;
    const lds_ptr sy = lds + L_SYNC;
    STAMP_DECL STAMP_RT(); STAMP();
    const int rb = (xcd & 3) * 4 + (idx & 3), c = (xcd >> 2) * 8 + (idx >> 2), j = c >> 1, odd = c & 1;
    const int rot = ((idx >> 2) + 2 * (idx & 3)) & 7;
    const int nh4 = ((j ^ c) & 1) == 0;
    const int hb0 = 7 * j + (odd ? (nh4 ? 3 : 4) : 0), hl0 = hb0 & ~1;
    const int head = xcd * 4 + (idx >> 3), qblk = idx & 7;
    const int mt = xcd * 4 + (idx >> 3), nt = idx & 7;
    u64 tag;
#define EPOCH_LOAD() asm volatile("s_load_dwordx2 %0, %1, 0x0" : "=&s"(tag) : "s"(ws + OFF_EP) : "memory")
#define EPOCH_WAIT() do { asm volatile("s_waitcnt lgkmcnt(0)" : "+s"(tag)::"memory"); tag += 1; } while (0)
    const __amdgpu_buffer_rsrc_t wsr = __builtin_amdgcn_make_buffer_rsrc(ws, 0, WS_USED, 0x00020000);
    const int wch = ((lane & 7) ^ (((lane >> 4) & 1) << 2)) * 16;
    const char* kg = ws + OFF_K + (size_t)head * 65536;
    const char* vg = ws + OFF_V + (size_t)head * 65536;

    if (w >= 8) {
        const int r8 = lane >> 3;
        const unsigned vx = (wm * 32 + r8) * 1024, cx0 = ((lane & 7) ^ (r8 >> 1)) << 4, cx1 = cx0 ^ 64;
        const unsigned vx0 = vx + cx0, vx1 = vx + 7168 + cx1, vx2 = vx + 2 * 7168 + cx0, vx3 = vx + 3 * 7168 + cx1;
        const unsigned vq = ((wm & 1) * 32 + r8) * 3072 + wch + (wm >> 1) * 1024, vh = ((wm & 1) * 32 + r8) * 3584 + wch + (wm >> 1) * 128;
        const char* xb = x + (size_t)rb * 128 * 1024;
        const char* qb = wqkv + j * 128 + odd * 1024;
        const char* hb = w1 + hl0 * 64;
        const unsigned rd0 = lds0 + L_RING + wm * 4096;
        auto unit = [&](int u) {
            const int ks = ((u >> 1) + rot) & 7;
            const unsigned d = rd0 + (u % NSLOT) * 16384;
            if (u & 1) glds4(xb + ks * 128, vx0, vx1, vx2, vx3, d);
            else if (u < 16) glds4(qb + ks * (64 * 3072), vq, vq + (8 * 3072 - 1024), vq + 2 * (8 * 3072 - 1024), vq + 3 * (8 * 3072 - 1024), d);
            else glds4(hb + ks * (64 * 3584), vh, vh + (8 * 3584 - 1024), vh + 2 * (8 * 3584 - 1024), vh + 3 * (8 * 3584 - 1024), d);
        };
        { const unsigned o1 = (lane & 11) == 0 ? 0x3f803f80u : 0u; *(LAS u32x4*)(lds + L_ONES + (tid - 512) * 16) = u32x4{o1, o1, o1, o1}; }
        if (w == 8) *(LAS int*)(sy + lane * 4) = 0;
        __builtin_amdgcn_s_setprio(1);
        unit(0); unit(1);
        EPOCH_LOAD();
        unit(2);
        EPOCH_WAIT();
        STAMP();
        VM_BAR(4);
        STAMP();
        unit(3); unit(4); unit(5); unit(6); unit(7);
        unsigned pf;
        {
            const int pt = bid < 224 ? bid : 223;
            const char* s2 = w2 + (size_t)(pt >> 3) * 65536 + (pt & 7) * 128;
            const unsigned v2 = (wm * 16 + r8) * 1024 + wch;
            glds16(s2, v2, lds0 + L_W2 + wm * 2048); glds16(s2 + 8192, v2, lds0 + L_W2 + wm * 2048 + 1024);
            if (wm & 1) glds1(b1 + hl0 * 64, lane * 4, lds0 + L_BIAS); else glds16(gate, lane * 16, lds0 + L_GATE);
            asm volatile("global_load_dword %0, %1, %2" : "=v"(pf) : "v"(((idx & 3) * 128 + wm * 32 + l31) * 3584 + h * 128), "s"(hb) : "memory");
        }
#pragma unroll
        for (int t = 1; t < 16; ++t) {
            if (t < 4) VM_BAR(20); else if (t < 14) VM_BAR(16); else if (t == 14) VM_BAR(8); else VM_BAR(0);
            if (2 * t + 6 < 32) { unit(2 * t + 6); unit(2 * t + 7); }
            if (t == 4 || t == 8 || t == 12) STAMP();
        }
        PIN(pf);
        STAMP();
        if (w == 8) { flag_wait<1>(ws, OFF_FQ + (head >> 2) * 256 + l31 * 8, tag); sig(sy, S_QOK, 1); }
        const char* kvg = (wm & 1) ? vg : kg;
        const unsigned kvd = lds0 + L_KV + (wm & 1) * 32768 + (wm >> 1) * 4096;
        asm volatile("s_barrier" ::: "memory");
        spin1(sy, S_QOK, 1);
        if (wm < 2) glds4c(ws + OFF_Q + (size_t)(head * SEQ + qblk * 256) * 32 + wm * 4096, lane * 16, lds0 + L_X + wm * 4096);
#pragma unroll
        for (int i = 0; i < 8; ++i) {
            const int hf = i < 2 ? i : (i < 5 ? 0 : 1), gi = i < 2 ? 0 : (i < 5 ? i - 1 : i - 4);
            glds4c(kvg + hf * 32768 + (((wm >> 1) + 2 * gi + qblk) & 7) * 4096, lane * 16, kvd + hf * 65536 + gi * 8192);
            if (i == 2) { VMW(8); sig(sy, S_KVM + wm * 4, 1); }
            if (i == 3) { VMW(8); sig(sy, S_KVM + wm * 4, 2); }
            if (i == 6) { VMW(8); sig(sy, S_KVM + wm * 4, 3); }
        }
        VMW(0); sig(sy, S_KVM + wm * 4, 4);
        STAMP();
        __builtin_amdgcn_s_setprio(3);
        if (w == 8) {
            flag_wait<1>(ws, h ? OFF_FH + ((mt >> 1) * 16 + (lane & 15)) * 8 : OFF_FW + (nt * 32 + (l31 < 28 ? l31 : 0)) * 8, tag);
            sig(sy, S_HOK, 1);
        } else spin1(sy, S_HOK, 1);
        STAMP();
        constexpr int PD = 4;
        const int hs = OFF_H + (mt * 2 * 112 + wm * 28) * 1024, wso = OFF_W2F + (nt * 2 * 112 + wm * 28) * 1024;
        u32x4 hf[PD][2], wf[PD][2];
        auto ld = [&](int i, int k) {
            hf[i][0] = WS_LOAD(lane * 16, hs + k * 1024); wf[i][0] = WS_LOAD(lane * 16, wso + k * 1024);
            hf[i][1] = WS_LOAD(lane * 16, hs + (112 + k) * 1024); wf[i][1] = WS_LOAD(lane * 16, wso + (112 + k) * 1024);
            SBAR();
        };
#pragma unroll
        for (int i = 0; i < PD; ++i) ld(i, i);
        f32x16 acc[2][2] = {};
#pragma unroll 1
        for (int k0 = 0; k0 < 28; k0 += PD) {
#pragma unroll
            for (int i = 0; i < PD; ++i) {
                const bool slow = RFL(*(LAS volatile int*)(sy + S_DONE)) < 8;
#pragma unroll
                for (int n = 0; n < 2; ++n)
#pragma unroll
                    for (int m = 0; m < 2; ++m) {
                        acc[n][m] = MFMA(BC8(wf[i][n]), BC8(hf[i][m]), acc[n][m]);
                        SBAR(); if (slow) __builtin_amdgcn_s_sleep(PACE); SBAR();
                    }
                const int kn = k0 + PD + i;
                ld(i, kn < 28 ? kn : 27);
            }
        }
        STAMP();
        spin1(sy, S_DONE, 8);
        LAS f32x4* pb = (LAS f32x4*)(lds + L_KV + 65536 + wm * 16384) + lane;
#pragma unroll
        for (int i = 0; i < 16; ++i) {
            const f32x16& a = acc[i >> 3][(i >> 2) & 1];
            pb[i * 64] = f32x4{a[(i & 3) * 4], a[(i & 3) * 4 + 1], a[(i & 3) * 4 + 2], a[(i & 3) * 4 + 3]};
        }
        LGW(); sig(sy, S_FB + wm * 4, 1);
        if (bid == 0 && w == 8) {
            flag_wait<4>(ws, OFF_FH + lane * 8, tag);
            flag_set(ws, OFF_EP, tag);
        }
        STAMP(); STAMP_RT(); STAMP_OUT(0, ws);
        return;
    }

    Geo g;
    g.lane = lane; g.w = w; g.wm = wm; g.h = h; g.l31 = l31; g.rot = rot;
    g.q = (lane & 15) >> 2; g.tcol = ((lane >> 4) & 1) * 32 + (lane & 3) * 8; g.sw = (g.q >> 1) << 6;
    const int tok0 = rb * 128 + wm * 32, half = w >> 2;
    EPOCH_LOAD();
    EPOCH_WAIT();
    __builtin_amdgcn_s_setprio(2);
    {
        f32x16 acc[2] = {};
        auto nohook = [] {};
        const unsigned o = (4 * j * SEQ + tok0 + l31) * 32 + h * 16;
        if (half) {
            proj_pass<1, false, false>(lds, g, odd ? 64 : 8192, acc, nohook, nohook);
            STAMP();
#pragma unroll
            for (int s = 0; s < 2; ++s) WS_STORE(pk8(acc[0], s), OFF_K + o + (2 * odd + s) * SEQ * 32);
        } else if (!odd) {
            proj_pass<2, false, false>(lds, g, 0, acc, nohook, nohook);
            STAMP();
#pragma unroll
            for (int i = 0; i < 4; ++i) WS_STORE(pk8(acc[i >> 1] * QSCALE, i & 1), OFF_Q + o + i * SEQ * 32);
        } else {
            proj_pass<2, true, false>(lds, g, 8192, acc, nohook, nohook);
            STAMP();
            const unsigned ov = OFF_V + ((4 * j + (l31 >> 4)) * 128 + (tok0 >> 4)) * 512 + h * 256 + (lane & 15) * 16;
#pragma unroll
            for (int i = 0; i < 4; ++i) WS_STORE(pk8(acc[i >> 1], i & 1), ov + (i >> 1) * 2 * 65536 + (i & 1) * 512);
        }
    }
    STAMP();
    {
        bool qpend = true;
        auto pubq_now = [&] {
            VMW(0);
            if (lds_inc(sy, S_CNTQ) == 7) flag_set(ws, OFF_FQ + ((j * 16 + rb) * 2 + odd) * 8, tag);
            qpend = false;
        };
        auto pubq = [&] { if (UNLIKELY(qpend && vm_idle())) pubq_now(); };
        const int pB = (nh4 || (hb0 & 1)) ? 1 : 0;
        const int blk = half ? 2 * pB : (nh4 ? 0 : (pB ? 1 : 2));
        const int nb = (half || nh4) ? 2 : 1;
        f32x16 acc[2];
        const int woff = (blk >> 1) * 8192 + (blk & 1) * 64;
        auto init2 = [&] { acc[0] = bias16(lds + L_BIAS + blk * 64 + h * 8); acc[1] = bias16(lds + L_BIAS + blk * 64 + 64 + h * 8); };
        auto init1 = [&] { acc[0] = bias16(lds + L_BIAS + blk * 64 + h * 8); acc[1] = f32x16{}; };
        if (nb == 2) proj_pass<2, false, false>(lds, g, woff, acc, init2, pubq);
        else if (c == 1) proj_pass<1, false, true>(lds, g, woff, acc, init1, pubq);
        else proj_pass<1, false, false>(lds, g, woff, acc, init1, pubq);
        LGKM_BAR();
        if (qpend) pubq_now();
        STAMP();
        const unsigned oh = OFF_H + (((tok0 >> 5) * 112 + (hl0 + blk) * 2) * 64 + lane) * 16;
        { const f32x16 v = mish16(acc[0]); WS_STORE(pk8(v, 0), oh); WS_STORE(pk8(v, 1), oh + 1024); }
        if (nb == 2) { const f32x16 v = mish16(acc[1]); WS_STORE(pk8(v, 0), oh + 2048); WS_STORE(pk8(v, 1), oh + 3072); }
        else if (c == 1) {
            const float e = __builtin_amdgcn_exp2f(acc[1][0] * -LOG2E);
            if (h == 0) __hip_atomic_store((GAS float*)(ws + OFF_G) + tok0 + l31, __builtin_amdgcn_rcpf(1.0f + e), __ATOMIC_RELAXED, __HIP_MEMORY_SCOPE_AGENT);
        }
        if (bid < 224) {
            const bf16x8 f = trd2(lds + L_W2 + (16 * wm + 4 * h + g.q) * 128 + g.tcol + (g.sw ^ (half * 64)), 1024);
            WS_STORE(__builtin_bit_cast(u32x4, f), OFF_W2F + ((((bid & 7) * 2 + half) * 112 + (bid >> 3) * 4 + wm) * 64 + lane) * 16);
        }
    }
    STAMP();
    bool hpend = true;
    auto pubh_now = [&] {
        VMW(0);
        if (lds_inc(sy, S_CNTH) == 7) {
            flag_set(ws, OFF_FH + (rb * 16 + c) * 8, tag);
            if (bid < 224) flag_set(ws, OFF_FW + ((bid & 7) * 32 + (bid >> 3)) * 8, tag);
        }
        hpend = false;
    };
    auto pubh = [&] { if (UNLIKELY(hpend && vm_idle())) pubh_now(); };
    while (min4(sy, S_KVM) < 1 + half) { pubh(); __builtin_amdgcn_s_sleep(1); }
    asm volatile("" ::: "memory");
    STAMP();
    const bf16x8 qA = ldsr128(lds + L_X + wm * 2048 + l31 * 32 + h * 16), qB = ldsr128(lds + L_X + wm * 2048 + 1024 + l31 * 32 + h * 16);
    const lds_cptr kp = lds + L_KV + half * 65536 + l31 * 32 + h * 16;
    const bool isone = (lane & 16) != 0;
    lds_cptr vp = lds + (isone ? L_ONES : L_KV + half * 65536 + 32768) + h * 256 + (lane & 15) * 16;
    const int vstep = isone ? 0 : 4096;
    f32x16 OA = {}, OB = {}, SA, SB;
    const f32x16 Z = {};
    bf16x8 kf = ldsr128(kp), v0 = ldsr128(vp), v1 = ldsr128(vp + 512);
    u32x4 p0, p1 = {0, 0, 0, 0};
    SA = MFMA(kf, qA, Z);
#define EX(i) S_[i] = __builtin_amdgcn_exp2f(S_[i])
#define UNIT(S, SN, QN, O, OP, LD0, LD1, LD2)                                                       \
    {                                                                                               \
        f32x16& S_ = S;                                                                             \
        SN = MFMA(kf, QN, Z); PIN(SN); SBAR();                                                      \
        LD0; EX(0); EX(1); PIN(S_); SBAR();                                                         \
        OP = MFMA(v1, BC8(p1), OP); PIN(OP); SBAR();                                                \
        LD1; EX(2); EX(3); EX(4); EX(5); EX(6); EX(7); PIN(S_); SBAR();                             \
        p0 = pk8(S_, 0); PIN(p0); SBAR();                                                           \
        EX(8); EX(9); PIN(S_); SBAR();                                                              \
        O = MFMA(v0, BC8(p0), O); PIN(O); SBAR();                                                   \
        LD2; EX(10); EX(11); EX(12); EX(13); EX(14); EX(15); PIN(S_); SBAR();                       \
        p1 = pk8(S_, 1); PIN(p1); SBAR();                                                           \
    }
#define TILE(t, u)                                                                                  \
    UNIT(SA, SB, qB, OA, OB, kf = ldsr128(kp + (((t) + 1) & 31) * 1024), v1 = ldsr128(vp + (u) * 1024 + 512), (void)0) \
    UNIT(SB, SA, qA, OB, OA, (void)0, (void)0, v0 = ldsr128((u) == 3 ? vp + vstep : vp + ((u) + 1) * 1024))
    __builtin_amdgcn_s_setprio(2);
#pragma unroll 1
    for (int t = 0; t < 32; t += 4) {
        if (t == 4) { lds_inc(sy, S_QRD); spin4(sy, S_KVM, 3 + half); }
        if (t == 12) __builtin_amdgcn_s_setprio(1);
        if (t == 24) __builtin_amdgcn_s_setprio(0);
        TILE(t, 0) pubh(); TILE(t + 1, 1) pubh(); TILE(t + 2, 2) pubh(); TILE(t + 3, 3) pubh();
        vp += vstep;
    }
    if (hpend) pubh_now();
    OB = MFMA(v1, BC8(p1), OB);
    PIN(kf); PIN(v0);
    LGW();
    lds_inc(sy, S_DONE);
    STAMP();
    spin1(sy, S_QRD, 8);
    {
        const f32x16 give = half ? OA : OB;
        LAS f32x4* dst = (LAS f32x4*)(lds + L_X + w * 2304) + lane;
        dst[0] = f32x4{give[0], give[1], give[2], give[3]};
        dst[64] = f32x4{give[4], give[5], give[6], give[7]};
        ((LAS float*)(lds + L_X + w * 2304 + 2048))[lane] = give[8];
        LGW(); sig(sy, S_FX + w * 4, 1);
    }
    f32x16 O = half ? OB : OA;
    spin1(sy, S_FX + (w ^ 4) * 4, 1);
    {
        LAS const f32x4* src = (LAS const f32x4*)(lds + L_X + (w ^ 4) * 2304) + lane;
        const f32x4 a = src[0], b = src[64];
#pragma unroll
        for (int r = 0; r < 4; ++r) { O[r] += a[r]; O[4 + r] += b[r]; }
        O[8] += ((LAS const float*)(lds + L_X + (w ^ 4) * 2304 + 2048))[lane];
    }
    bool bad = !(O[8] > 0x1p-90f && O[8] < 0x1p100f);
#pragma unroll
    for (int r = 0; r < 8; ++r) bad |= !(__builtin_fabsf(O[r]) < 0x1p120f);
    if (__any(bad)) {
        const bf16x8 qf = half ? qB : qA;
        const unsigned o1 = (lane & 11) == 0 ? 0x3f803f80u : 0u;
        float m = -INFINITY;
        O = Z;
#pragma unroll 1
        for (int t = 0; t < 64; ++t) {
            f32x16 s = MFMA(BC8(WS_LOAD(l31 * 32 + h * 16, OFF_K + head * 65536 + t * 1024)), qf, Z);
            u32x4 va = WS_LOAD(h * 256 + (lane & 15) * 16, OFF_V + head * 65536 + t * 1024);
            u32x4 vb = WS_LOAD(h * 256 + (lane & 15) * 16, OFF_V + head * 65536 + t * 1024 + 512);
            if (isone) { va = u32x4{o1, o1, o1, o1}; vb = va; }
            float tm = s[0];
#pragma unroll
            for (int r = 1; r < 16; ++r) tm = __builtin_fmaxf(tm, s[r]);
            tm = __builtin_fmaxf(tm, __shfl_xor(tm, 32));
            const float mn = __builtin_fmaxf(m, tm);
            const float al = __builtin_amdgcn_exp2f(m - mn);
            m = mn;
#pragma unroll
            for (int r = 0; r < 16; ++r) { O[r] *= al; s[r] = __builtin_amdgcn_exp2f(s[r] - mn); }
            O = MFMA(BC8(va), BC8(pk8(s, 0)), O);
            O = MFMA(BC8(vb), BC8(pk8(s, 1)), O);
        }
    }
    WS_STORE(pk8(O * __builtin_amdgcn_rcpf(O[8]), 0), OFF_O + (((qblk * 8 + wm * 2 + half) * 32 + head) * 64 + lane) * 16);
    spin1(sy, S_DONE, 8);
    const char* wg = wo + nt * 128 + (size_t)w * 65536;
    const unsigned vow = (lane >> 3) * 1024 + wch;
    glds4(wg, vow, vow + 7168, vow + 2 * 7168, vow + 3 * 7168, lds0 + L_KV + w * 8192);
    VMW(4);
    if (lds_inc(sy, S_CNTO) == 7) flag_set(ws, OFF_FO + (qblk * 32 + head) * 8, tag);
    STAMP();
    glds4(wg + 32768, vow, vow + 7168, vow + 2 * 7168, vow + 3 * 7168, lds0 + L_KV + w * 8192 + 4096);
    const int wn = (w >> 1) & 1, wmm = w & 1;
    const int row = mt * 64 + wmm * 32 + l31;
    f32x16 bq = {};
    float gv = 0.f;
    if (w < 4) {
        spin4(sy, S_FB, 1);
        gv = __hip_atomic_load((GAS float*)(ws + OFF_G) + row, __ATOMIC_RELAXED, __HIP_MEMORY_SCOPE_AGENT);
#pragma unroll
        for (int p = 0; p < 4; ++p)
#pragma unroll
            for (int i = 0; i < 4; ++i) {
                const f32x4 r = ((LAS const f32x4*)(lds + L_KV + 65536 + p * 16384 + wm * 4096))[i * 64 + lane];
#pragma unroll
                for (int e = 0; e < 4; ++e) bq[4 * i + e] += r[e];
            }
    }
    VMW(0); sig(sy, S_WOF + w * 4, 1);
    STAMP();
    if (w == 4) { flag_wait<1>(ws, OFF_FO + (mt >> 2) * 256 + l31 * 8, tag); sig(sy, S_OOK, 1); } else spin1(sy, S_OOK, 1);
    STAMP();
    u32x4 of[16];
#pragma unroll
    for (int i = 0; i < 16; ++i) of[i] = WS_LOAD(lane * 16, OFF_O + ((mt * 2 + wmm) * 32 + half * 16 + i) * 1024);
    spin4(sy, S_WOF + half * 16, 1);
    const lds_cptr wa = lds + L_KV + half * 32768 + (4 * h + g.q) * 128 + (g.sw ^ (wn * 64)) + g.tcol;
    f32x16 acc = {};
#pragma unroll
    for (int i = 0; i < 16; ++i) acc = MFMA(trd2(wa + i * 2048, 1024), BC8(of[i]), acc);
    STAMP();
    LAS f32x4* ex = (LAS f32x4*)(lds + L_X + wm * 4096) + lane;
    if (w >= 4) {
        spin4(sy, S_WOF, 1);
#pragma unroll
        for (int i = 0; i < 4; ++i) ex[i * 64] = f32x4{acc[4 * i], acc[4 * i + 1], acc[4 * i + 2], acc[4 * i + 3]};
        LGW(); sig(sy, S_FY + wm * 4, 1);
    } else {
        spin1(sy, S_FY + wm * 4, 1);
#pragma unroll
        for (int i = 0; i < 4; ++i) {
            const f32x4 r = ex[i * 64];
#pragma unroll
            for (int e = 0; e < 4; ++e) acc[4 * i + e] += r[e];
        }
        const f32x16 res = acc * gv + bq * (1.0f - gv);
        const u32x4 lo = pk8(res, 0), hi = pk8(res, 1);
#pragma unroll
        for (int k = 0; k < 2; ++k) {
            const u32x4 o = k ? hi : lo;
            auto r0 = __builtin_amdgcn_permlane32_swap(o[0], o[2], false, false);
            auto r1 = __builtin_amdgcn_permlane32_swap(o[1], o[3], false, false);
            *(GAS u32x4*)(out + (size_t)row * 1024 + (nt * 64 + wn * 32 + 16 * k + 8 * h) * 2) = u32x4{r0[0], r1[0], r0[1], r1[1]};
        }
    }
    STAMP(); STAMP_RT(); STAMP_OUT(0, ws);
}

extern "C" void kernel_launch(void* const* d_in, const int* in_sizes, int n_in, void* d_out, int out_size, void* d_ws, size_t ws_size,
                              hipStream_t stream) {
    fused_block<<<256, 768, 0, stream>>>((const char*)d_in[0], (const char*)d_in[1], (const char*)d_in[2], (const char*)d_in[4],
                                         (const char*)d_in[5], (const char*)d_in[6], (const char*)d_in[7], (char*)d_ws, (char*)d_out);
}
```

```cpp
#include <hip/hip_runtime.h>
#include <hip/hip_bf16.h>
#include <stdint.h>

typedef short bf16x8 __attribute__((ext_vector_type(8)));
typedef short s16x4 __attribute__((ext_vector_type(4)));
typedef float f32x16 __attribute__((ext_vector_type(16)));
typedef float f32x2 __attribute__((ext_vector_type(2)));
typedef float f32x4 __attribute__((ext_vector_type(4)));
typedef __bf16 bf16x2_t __attribute__((ext_vector_type(2)));
typedef unsigned u32x4 __attribute__((ext_vector_type(4)));
typedef unsigned u32x2 __attribute__((ext_vector_type(2)));
typedef int i32x4 __attribute__((ext_vector_type(4)));
typedef unsigned long long u64;
#define LAS __attribute__((address_space(3)))
#define GAS __attribute__((address_space(1)))
typedef LAS const char* lds_cptr;
typedef LAS char* lds_ptr;
#define DI __device__ __forceinline__
#define MFMA(a, b, c) __builtin_amdgcn_mfma_f32_32x32x16_bf16((a), (b), (c), 0, 0, 0)
#define SBAR() __builtin_amdgcn_sched_barrier(0)
#define PIN(x) asm volatile("" : "+v"(x))
#define VMW(N) asm volatile("s_waitcnt vmcnt(%0)" ::"n"(N) : "memory")
#define LGKM_BAR() asm volatile("s_waitcnt lgkmcnt(0)\n\ts_barrier" ::: "memory")
#define VM_BAR(N) asm volatile("s_waitcnt vmcnt(%0)\n\ts_barrier" ::"n"(N) : "memory")
#define LGW() asm volatile("s_waitcnt lgkmcnt(0)" ::: "memory")
#define BC8(x) __builtin_bit_cast(bf16x8, (x))
#define RFL(x) __builtin_amdgcn_readfirstlane(x)

constexpr int SEQ = 2048;
constexpr unsigned MB = 1024 * 1024;
constexpr unsigned OFF_Q = 0, OFF_K = 2 * MB, OFF_V = 4 * MB, OFF_H = 6 * MB, OFF_W2F = 14 * MB, OFF_O = 16 * MB, OFF_G = 18 * MB,
                   OFF_FQ = 18 * MB + 16384, OFF_FH = OFF_FQ + 2048, OFF_FO = OFF_FH + 2048, OFF_FW = OFF_FO + 2048, OFF_EP = OFF_FW + 2048,
                   WS_USED = 19 * MB;
constexpr unsigned POISON = 0xffffffffu;
constexpr float LOG2E = 1.4426950408889634f;
constexpr float QSCALE = 0.25f * LOG2E;

constexpr int L_KV = 0;
constexpr int L_RING = 0, NSLOT = 8;
constexpr int L_X = 131072;
constexpr int L_W2 = 147456, L_GATE = 155648, L_ONES = 156672, L_SYNC = 160768, L_TOTAL = L_SYNC + 512;
constexpr int L_BIAS = L_SYNC + 256;
constexpr int S_QRD = 0, S_P24 = 4, S_EX = 16,  S_KVM = 48, S_QOK = 80, S_CNTQ = 84, S_CNTH = 88, S_DONE = 96, S_P16 = 100,
              S_HOK = 104, S_FX = 112  , S_FB = 144, S_WOF = 160, S_FY = 192, S_FM = 208  , S_OF = 64  , S_OF2 = 240  ;

#define TRACE(kid, i)
#define STAMP_DECL
#define STAMP()
#define STAMP1()
#define STAMP_RT()
#define STAMP_OUT(kid, wsp)

DI unsigned pk2(float a, float b) {
    f32x2 v = {a, b};
    return __builtin_bit_cast(unsigned, __builtin_convertvector(v, bf16x2_t));
}
DI u32x4 pk8(const f32x16& x, int s) {
    u32x4 r = {pk2(x[8 * s], x[8 * s + 1]), pk2(x[8 * s + 2], x[8 * s + 3]), pk2(x[8 * s + 4], x[8 * s + 5]), pk2(x[8 * s + 6], x[8 * s + 7])};
    return r;
}
DI float bflo(unsigned u) { return __uint_as_float(u << 16); }
DI float bfhi(unsigned u) { return __uint_as_float(u & 0xffff0000u); }
DI void glds16(const void* sbase, unsigned voff, unsigned lds_dst) {
    unsigned sv;
    asm volatile("s_mov_b32 %0, m0\n\ts_mov_b32 m0, %3\n\ts_nop 0\n\tglobal_load_lds_dwordx4 %1, %2\n\ts_mov_b32 m0, %0"
                 : "=&s"(sv) : "v"(voff), "s"(sbase), "s"(lds_dst) : "memory");
}
DI void glds1(const void* sbase, unsigned voff, unsigned lds_dst) {
    unsigned sv;
    asm volatile("s_mov_b32 %0, m0\n\ts_mov_b32 m0, %3\n\ts_nop 0\n\tglobal_load_lds_dword %1, %2\n\ts_mov_b32 m0, %0"
                 : "=&s"(sv) : "v"(voff), "s"(sbase), "s"(lds_dst) : "memory");
}
#define GLDS4_BODY(POL)                                                                                                    \
    unsigned sv;                                                                                                           \
    asm volatile("s_mov_b32 %0, m0\n\ts_mov_b32 m0, %6\n\ts_nop 0\n\tglobal_load_lds_dwordx4 %1, %5" POL                   \
                 "\n\tglobal_load_lds_dwordx4 %2, %5 offset:1024" POL "\n\tglobal_load_lds_dwordx4 %3, %5 offset:2048" POL \
                 "\n\tglobal_load_lds_dwordx4 %4, %5 offset:3072" POL "\n\ts_mov_b32 m0, %0"                               \
                 : "=&s"(sv) : "v"(v0), "v"(v1), "v"(v2), "v"(v3), "s"(sbase), "s"(lds_dst) : "memory")
DI void glds4(const void* sbase, unsigned v0, unsigned v1, unsigned v2, unsigned v3, unsigned lds_dst) { GLDS4_BODY(""); }
DI void glds4c(const void* sbase, unsigned v0, unsigned lds_dst) { const unsigned v1 = v0, v2 = v0, v3 = v0; GLDS4_BODY(" sc1"); }
DI bf16x8 ldsr128(lds_cptr p) { return *(LAS const bf16x8*)p; }
DI s16x4 trd(lds_cptr p) { return __builtin_amdgcn_ds_read_tr16_b64_v4i16((LAS s16x4*)p); }
DI bf16x8 trd2(lds_cptr p, int second) {
    s16x4 lo = trd(p), hi = trd(p + second);
    return __builtin_shufflevector(lo, hi, 0, 1, 2, 3, 4, 5, 6, 7);
}
DI f32x16 mish16(const f32x16& h) {
    f32x16 e = h * LOG2E;
#pragma unroll
    for (int i = 0; i < 16; ++i) e[i] = __builtin_amdgcn_exp2f(e[i]);
    const f32x16 mh = {-0.5f, -0.5f, -0.5f, -0.5f, -0.5f, -0.5f, -0.5f, -0.5f, -0.5f, -0.5f, -0.5f, -0.5f, -0.5f, -0.5f, -0.5f, -0.5f};
    const f32x16 m1 = mh + mh;
    f32x16 d = __builtin_elementwise_fma(e, __builtin_elementwise_fma(e, mh, m1), m1);
#pragma unroll
    for (int i = 0; i < 16; ++i) d[i] = __builtin_amdgcn_rcpf(d[i]);
    return __builtin_elementwise_fma(h, d, h);
}
DI bool vm_idle() {
    unsigned r;
    asm volatile("s_getreg_b32 %0, hwreg(HW_REG_IB_STS, 0, 4)" : "=s"(r)::"memory");
    return r == 0;
}
DI void sig(lds_ptr sy, int off, int v) {
    asm volatile("s_mov_b64 exec, 1\n\tds_write_b32 %0, %1\n\ts_mov_b64 exec, -1" ::"v"((unsigned)(uintptr_t)(sy + off)), "v"(v) : "memory");
}
#define UNLIKELY(x) __builtin_expect(!!(x), 0)
DI void spin1(lds_ptr sy, int off, int need) {
    while (RFL(*(LAS volatile int*)(sy + off)) < need) __builtin_amdgcn_s_sleep(1);
    asm volatile("" ::: "memory");
}
DI int min4(lds_ptr sy, int off) {
    const i32x4 v = *(LAS volatile i32x4*)(sy + off);
    return RFL(min(min(v[0], v[1]), min(v[2], v[3])));
}
DI int min8(lds_ptr sy, int off) {
    const i32x4 v = *(LAS volatile i32x4*)(sy + off), u = *(LAS volatile i32x4*)(sy + off + 16);
    return RFL(min(min(min(v[0], v[1]), min(v[2], v[3])), min(min(u[0], u[1]), min(u[2], u[3]))));
}
DI void spin4(lds_ptr sy, int off, int need) {
    while (min4(sy, off) < need) __builtin_amdgcn_s_sleep(1);
    asm volatile("" ::: "memory");
}
DI void spin4f(lds_ptr sy, int off, int need) {
    while (min4(sy, off) < need) {}
    asm volatile("" ::: "memory");
}
DI void lds_add1(lds_ptr sy, int off) {
    asm volatile("s_mov_b64 exec, 1\n\tds_add_u32 %0, %1\n\ts_mov_b64 exec, -1" ::"v"((unsigned)(uintptr_t)(sy + off)), "v"(1) : "memory");
}
DI int lds_inc(lds_ptr sy, int off) {
    int r = 0;
    if ((threadIdx.x & 63) == 0) r = __hip_atomic_fetch_add((LAS int*)(sy + off), 1, __ATOMIC_RELAXED, __HIP_MEMORY_SCOPE_WORKGROUP);
    return RFL(r);
}
#define POLL_SLEEP 2
DI void flag_set(char* ws, unsigned off, u64 tag) {
    if ((threadIdx.x & 63) == 0) __hip_atomic_store((GAS u64*)(ws + off), tag, __ATOMIC_RELAXED, __HIP_MEMORY_SCOPE_AGENT);
}
template <int N>
DI void flag_wait(char* ws, unsigned laneoff, u64 tag) {
    GAS u64* p = (GAS u64*)(ws + laneoff);
    for (int spins = 0; spins < (1 << 20); ++spins) {
        bool ok = true;
#pragma unroll
        for (int i = 0; i < N; ++i) ok &= __hip_atomic_load(p + i * 64, __ATOMIC_RELAXED, __HIP_MEMORY_SCOPE_AGENT) == tag;
        if (__all(ok)) break;
        __builtin_amdgcn_s_sleep(POLL_SLEEP);
    }
    asm volatile("" ::: "memory");
}
#define WS_STORE(v, off) __builtin_amdgcn_raw_buffer_store_b128((v), wsr, (off), 0, 16)
#define WS_LOAD(voff, soff) __builtin_amdgcn_raw_buffer_load_b128(wsr, (voff), (soff), 16)

struct Geo {
    int lane, w, wm, h, l31, rot, q, tcol, sw;
};

template <int NB, bool SWAP, bool GATE, class Init, class Hook>
DI void proj_pass(lds_ptr lds, const Geo& g, int woff, f32x16 (&acc)[2], Init init, Hook hook) {
    const int xr = g.wm * 32 + g.l31;
    int xo[4];
#pragma unroll
    for (int s = 0; s < 4; ++s) xo[s] = L_RING + 16384 + xr * 128 + (((2 * s + g.h) ^ ((xr >> 1) & 7)) << 4);
    const int w0 = (L_RING + (8 * g.h + g.q) * 128 + g.sw + g.tcol) ^ woff, w1 = w0 ^ 64;
    constexpr int NF = NB + (GATE ? 1 : 0);
    bf16x8 xf[4], wf[4][NF];
    auto rd = [&](int s, int b, int t) {
        const int so = (t & 3) * 32768;
        if (b == 0) xf[s] = ldsr128(lds + xo[s] + so);
        if (b < NB) wf[s][b] = trd2(lds + ((b ? w1 : w0) + so) + s * 2048, 512);
        else wf[s][b] = ldsr128(lds + L_GATE + ((t + g.rot) & 7) * 128 + g.h * 16 + s * 32);
        SBAR();
    };
    auto mm = [&](int s, int b) {
        acc[b] = SWAP ? MFMA(xf[s], wf[s][b], acc[b]) : MFMA(wf[s][b], xf[s], acc[b]);
        SBAR();
    };
    LGKM_BAR(); SBAR();
    init();
#pragma unroll
    for (int s = 0; s < 4; ++s)
#pragma unroll
        for (int b = 0; b < NF; ++b) { if (s >= 2) mm(s - 2, b); rd(s, b, 0); }
#pragma unroll 1
    for (int t = 1; t < 8; ++t) {
        hook();
        LGKM_BAR(); SBAR();
#pragma unroll
        for (int s = 0; s < 4; ++s)
#pragma unroll
            for (int b = 0; b < NF; ++b) { mm((s + 2) & 3, b); rd(s, b, t); }
    }
#pragma unroll
    for (int s = 2; s < 4; ++s)
#pragma unroll
        for (int b = 0; b < NF; ++b) mm(s, b);
}
DI f32x16 bias16(lds_cptr p) {
    f32x16 v;
#pragma unroll
    for (int i = 0; i < 4; ++i) {
        const u32x2 bb = *(LAS const u32x2*)(p + 16 * i);
        v[4 * i] = bflo(bb[0]); v[4 * i + 1] = bfhi(bb[0]); v[4 * i + 2] = bflo(bb[1]); v[4 * i + 3] = bfhi(bb[1]);
    }
    return v;
}

#define LOOKA 8
#define PACE 2

__global__ __launch_bounds__(768, 3) void fused_block(const char* __restrict__ x, const char* __restrict__ wqkv, const char* __restrict__ wo,
                                                      const char* __restrict__ w1, const char* __restrict__ b1, const char* __restrict__ w2,
                                                      const char* __restrict__ gate, char* __restrict__ ws, char* __restrict__ out) {
    __shared__ __attribute__((aligned(1024))) char ldsmem[L_TOTAL];
    const int tid = threadIdx.x, lane = tid & 63, w = RFL(tid >> 6), wm = w & 3, h = lane >> 5, l31 = lane & 31;
    const int bid = blockIdx.x, xcd = bid & 7, idx = bid >> 3;
    const unsigned lds0 = (unsigned)(uintptr_t)ldsmem;
    const lds_ptr lds = (lds_ptr)ldsmem;
    const lds_ptr sy = lds + L_SYNC;
    STAMP_DECL STAMP_RT(); STAMP();
    u64 code_pc;
    unsigned code_len;
    asm volatile("s_getpc_b64 %0\n.Lcode_here:\n\ts_max_i32 %1, .Lcode_end_a-.Lcode_here, 0\n\ts_max_i32 %1, .Lcode_end_b-.Lcode_here, %1"
                 : "=s"(code_pc), "=&s"(code_len)::"scc");
    const int rb = (xcd & 3) * 4 + (idx & 3), c = (xcd >> 2) * 8 + (idx >> 2), j = c >> 1, odd = c & 1;
    const int rot = ((idx >> 2) + 2 * (idx & 3)) & 7;
    const int nh4 = ((j ^ c) & 1) == 0;
    const int hb0 = 7 * j + (odd ? (nh4 ? 3 : 4) : 0), hl0 = hb0 & ~1;
    const int head = xcd * 4 + (idx >> 3), qblk = idx & 7;
    const int mt = xcd * 4 + (idx >> 3), nt = idx & 7;
    u64 tag;
#define EPOCH_LOAD() asm volatile("s_load_dwordx2 %0, %1, 0x0" : "=&s"(tag) : "s"(ws + OFF_EP) : "memory")
#define EPOCH_WAIT() do { asm volatile("s_waitcnt lgkmcnt(0)" : "+s"(tag)::"memory"); tag += 1; } while (0)
    const __amdgpu_buffer_rsrc_t wsr = __builtin_amdgcn_make_buffer_rsrc(ws, 0, WS_USED, 0x00020000);
    const int wch = ((lane & 7) ^ (((lane >> 4) & 1) << 2)) * 16;
    const char* kg = ws + OFF_K + (size_t)head * 65536;
    const char* vg = ws + OFF_V + (size_t)head * 65536;

    if (__builtin_expect(w >= 8, 1)) {
        const int r8 = lane >> 3;
        const unsigned vx = (wm * 32 + r8) * 1024, cx0 = ((lane & 7) ^ (r8 >> 1)) << 4, cx1 = cx0 ^ 64;
        const unsigned vx0 = vx + cx0, vx1 = vx + 7168 + cx1, vx2 = vx + 2 * 7168 + cx0, vx3 = vx + 3 * 7168 + cx1;
        const unsigned vq = ((wm & 1) * 32 + r8) * 3072 + wch + (wm >> 1) * 1024, vh = ((wm & 1) * 32 + r8) * 3584 + wch + (wm >> 1) * 128;
        const char* xb = x + (size_t)rb * 128 * 1024;
        const char* qb = wqkv + j * 128 + odd * 1024;
        const char* hb = w1 + hl0 * 64;
        const unsigned rd0 = lds0 + L_RING + wm * 4096;
        auto unit = [&](int u) {
            const int ks = ((u >> 1) + rot) & 7;
            const unsigned d = rd0 + (u % NSLOT) * 16384;
            if (u & 1) glds4(xb + ks * 128, vx0, vx1, vx2, vx3, d);
            else if (u < 16) glds4(qb + ks * (64 * 3072), vq, vq + (8 * 3072 - 1024), vq + 2 * (8 * 3072 - 1024), vq + 3 * (8 * 3072 - 1024), d);
            else glds4(hb + ks * (64 * 3584), vh, vh + (8 * 3584 - 1024), vh + 2 * (8 * 3584 - 1024), vh + 3 * (8 * 3584 - 1024), d);
        };
        { const unsigned o1 = (lane & 11) == 0 ? 0x3f803f80u : 0u; *(LAS u32x4*)(lds + L_ONES + (tid - 512) * 16) = u32x4{o1, o1, o1, o1}; }
        if (w == 8) *(LAS int*)(sy + lane * 4) = 0;
        __builtin_amdgcn_s_setprio(1);
        unit(0); unit(1);
        EPOCH_LOAD();
        unit(2);
        EPOCH_WAIT();
        STAMP();
        VM_BAR(4);
        STAMP();
        unit(3); unit(4); unit(5); unit(6); unit(7);
        unsigned pf;
        {
            const int pt = bid < 224 ? bid : 223;
            const char* s2 = w2 + (size_t)(pt >> 3) * 65536 + (pt & 7) * 128;
            const unsigned v2 = (wm * 16 + r8) * 1024 + wch;
            glds16(s2, v2, lds0 + L_W2 + wm * 2048); glds16(s2 + 8192, v2, lds0 + L_W2 + wm * 2048 + 1024);
            if (wm & 1) glds1(b1 + hl0 * 64, lane * 4, lds0 + L_BIAS); else glds16(gate, lane * 16, lds0 + L_GATE);
            asm volatile("global_load_dword %0, %1, %2" : "=v"(pf) : "v"(((idx & 3) * 128 + wm * 32 + l31) * 3584 + h * 128), "s"(hb) : "memory");
        }
#pragma unroll
        for (int t = 1; t < 16; ++t) {
            if (t < 4) VM_BAR(20); else if (t < 14) VM_BAR(16); else if (t == 14) VM_BAR(8); else VM_BAR(0);
            if (2 * t + 6 < 32) { unit(2 * t + 6); unit(2 * t + 7); }
            if (t == 4 || t == 8 || t == 12) STAMP();
        }
        PIN(pf);
        STAMP();
        if (w == 8) { flag_wait<1>(ws, OFF_FQ + (head >> 2) * 256 + l31 * 8, tag); sig(sy, S_QOK, 1); }
        const char* kvg = (wm & 1) ? vg : kg;
        const unsigned kvd = lds0 + L_KV + (wm & 1) * 32768 + (wm >> 1) * 4096;
        asm volatile("s_barrier" ::: "memory");
        spin1(sy, S_QOK, 1);
        if (wm < 2) glds4c(ws + OFF_Q + (size_t)(head * SEQ + qblk * 256) * 32 + wm * 4096, lane * 16, lds0 + L_X + wm * 4096);
#pragma unroll
        for (int i = 0; i < 8; ++i) {
            const int hf = i < 2 ? i : (i < 5 ? 0 : 1), gi = i < 2 ? 0 : (i < 5 ? i - 1 : i - 4);
            glds4c(kvg + hf * 32768 + (((wm >> 1) + 2 * gi + qblk) & 7) * 4096, lane * 16, kvd + hf * 65536 + gi * 8192);
            if (i == 2) { VMW(8); sig(sy, S_KVM + wm * 4, 1); }
            if (i == 3) { VMW(8); sig(sy, S_KVM + wm * 4, 2); }
            if (i == 6) { VMW(8); sig(sy, S_KVM + wm * 4, 3); }
        }
        VMW(0); sig(sy, S_KVM + wm * 4, 4);
        STAMP();
        __builtin_amdgcn_s_setprio(3);
        if (w == 8) {
            flag_wait<1>(ws, h ? OFF_FH + ((mt >> 1) * 16 + (lane & 15)) * 8 : OFF_FW + (nt * 32 + (l31 < 28 ? l31 : 0)) * 8, tag);
            sig(sy, S_HOK, 1);
        } else spin1(sy, S_HOK, 1);
        STAMP();
        constexpr int PD = 4;
        const int hs = OFF_H + ((mt * 2 + (wm & 1)) * 112 + wm * 28) * 1024, hs1 = OFF_H + ((mt * 2 + ((wm & 1) ^ 1)) * 112 + wm * 28) * 1024;
        const int wso = OFF_W2F + ((nt * 2 + (wm >> 1)) * 112 + wm * 28) * 1024, wso1 = OFF_W2F + ((nt * 2 + ((wm >> 1) ^ 1)) * 112 + wm * 28) * 1024;
        u32x4 hf[PD][2], wf[PD][2];
        auto ld = [&](int i, int k) {
            hf[i][0] = WS_LOAD(lane * 16, hs + k * 1024); wf[i][0] = WS_LOAD(lane * 16, wso + k * 1024);
            hf[i][1] = WS_LOAD(lane * 16, hs1 + k * 1024); wf[i][1] = WS_LOAD(lane * 16, wso1 + k * 1024);
            SBAR();
        };
#pragma unroll
        for (int i = 0; i < PD; ++i) ld(i, i);
        f32x16 acc[2][2] = {};
        bool wopend = true;
        auto load_wo = [&] {
            const char* wg = wo + nt * 128 + (size_t)wm * 131072;
            const unsigned vow = r8 * 1024 + wch;
#pragma unroll
            for (int i = 0; i < 4; ++i) glds4(wg + i * 32768, vow, vow + 7168, vow + 2 * 7168, vow + 3 * 7168, lds0 + L_KV + wm * 32768 + i * 4096);
            wopend = false;
        };
#pragma unroll 1
        for (int k0 = 0; k0 < 28; k0 += PD) {
#pragma unroll
            for (int i = 0; i < PD; ++i) {
                const u32x2 st2 = *(LAS volatile u32x2*)(sy + S_DONE);
                const bool slow = RFL(st2[0]) < 8;
                if (UNLIKELY(wopend && RFL(st2[1]) == 8)) load_wo();
#pragma unroll
                for (int n = 0; n < 2; ++n)
#pragma unroll
                    for (int m = 0; m < 2; ++m) {
                        acc[n][m] = MFMA(BC8(wf[i][n]), BC8(hf[i][m]), acc[n][m]);
                        SBAR(); if (slow) __builtin_amdgcn_s_sleep(PACE); SBAR();
                    }
                const int kn = k0 + PD + i;
                ld(i, kn < 28 ? kn : 27);
            }
        }
        STAMP();
        if (wopend) { spin1(sy, S_P16, 8); load_wo(); }
        VMW(0);
        spin1(sy, S_P24, 8);
        f32x16 keep = acc[0][0];
#pragma unroll
        for (int r = 1; r < 4; ++r) {
            const f32x16& gv = acc[r >> 1][r & 1];
            if (r == 3) spin1(sy, S_EX + (wm ^ 1) * 4, 2);
            LAS f32x4* dst = (LAS f32x4*)(lds + L_KV + wm * 32768 + 16384 + (r & 1) * 4096) + lane;
#pragma unroll
            for (int i = 0; i < 4; ++i) dst[i * 64] = f32x4{gv[4 * i], gv[4 * i + 1], gv[4 * i + 2], gv[4 * i + 3]};
            LGW(); sig(sy, S_EX + wm * 4, r);
            spin1(sy, S_EX + (wm ^ r) * 4, r);
            LAS const f32x4* src = (LAS const f32x4*)(lds + L_KV + (wm ^ r) * 32768 + 16384 + (r & 1) * 4096) + lane;
#pragma unroll
            for (int i = 0; i < 4; ++i) {
                const f32x4 v = src[i * 64];
#pragma unroll
                for (int e = 0; e < 4; ++e) keep[4 * i + e] += v[e];
            }
        }
        { LAS u32x4* pb = (LAS u32x4*)(lds + L_W2 + wm * 2048) + lane; pb[0] = pk8(keep, 0); pb[64] = pk8(keep, 1); }
        LGW(); sig(sy, S_FB + wm * 4, 1);
        if (bid == 0 && w == 8) {
            flag_wait<4>(ws, OFF_FH + lane * 8, tag);
            flag_set(ws, OFF_EP, tag);
        }
        STAMP(); STAMP_RT(); STAMP_OUT(0, ws);
        asm volatile(".Lcode_end_a:");
        return;
    }

    Geo g;
    g.lane = lane; g.w = w; g.wm = wm; g.h = h; g.l31 = l31; g.rot = rot;
    g.q = (lane & 15) >> 2; g.tcol = ((lane >> 4) & 1) * 32 + (lane & 3) * 8; g.sw = (g.q >> 1) << 6;
    const int tok0 = rb * 128 + wm * 32, half = w >> 2;
    unsigned cpf = 0;
    if (w == 0) {
        const unsigned o = (lane * 32 + idx) * 128;
        if (o < code_len) asm volatile("global_load_dword %0, %1, %2" : "=v"(cpf) : "v"(o), "s"(code_pc) : "memory");
    }
    EPOCH_LOAD();
    EPOCH_WAIT();
    __builtin_amdgcn_s_setprio(2);
    {
        f32x16 acc[2] = {};
        auto nohook = [] {};
        const unsigned o = (4 * j * SEQ + tok0 + l31) * 32 + h * 16;
        if (half) {
            proj_pass<1, false, false>(lds, g, odd ? 64 : 8192, acc, nohook, nohook);
            STAMP();
#pragma unroll
            for (int s = 0; s < 2; ++s) WS_STORE(pk8(acc[0], s), OFF_K + o + (2 * odd + s) * SEQ * 32);
        } else if (!odd) {
            proj_pass<2, false, false>(lds, g, 0, acc, nohook, nohook);
            STAMP();
#pragma unroll
            for (int i = 0; i < 4; ++i) WS_STORE(pk8(acc[i >> 1] * QSCALE, i & 1), OFF_Q + o + i * SEQ * 32);
        } else {
            proj_pass<2, true, false>(lds, g, 8192, acc, nohook, nohook);
            STAMP();
            const unsigned ov = OFF_V + ((4 * j + (l31 >> 4)) * 128 + (tok0 >> 4)) * 512 + h * 256 + (lane & 15) * 16;
#pragma unroll
            for (int i = 0; i < 4; ++i) WS_STORE(pk8(acc[i >> 1], i & 1), ov + (i >> 1) * 2 * 65536 + (i & 1) * 512);
        }
    }
    WS_STORE((u32x4{POISON, POISON, POISON, POISON}), OFF_O + (((rb * 4 + wm) * 32 + 4 * j + 2 * odd + half) * 64 + lane) * 16);
    STAMP();
    {
        bool qpend = true;
        auto pubq_now = [&] {
            VMW(0); PIN(cpf);
            if (lds_inc(sy, S_CNTQ) == 7) flag_set(ws, OFF_FQ + ((j * 16 + rb) * 2 + odd) * 8, tag);
            qpend = false;
        };
        auto pubq = [&] { if (UNLIKELY(qpend && vm_idle())) pubq_now(); };
        const int pB = (nh4 || (hb0 & 1)) ? 1 : 0;
        const int blk = half ? 2 * pB : (nh4 ? 0 : (pB ? 1 : 2));
        const int nb = (half || nh4) ? 2 : 1;
        f32x16 acc[2];
        const int woff = (blk >> 1) * 8192 + (blk & 1) * 64;
        auto init2 = [&] { acc[0] = bias16(lds + L_BIAS + blk * 64 + h * 8); acc[1] = bias16(lds + L_BIAS + blk * 64 + 64 + h * 8); };
        auto init1 = [&] { acc[0] = bias16(lds + L_BIAS + blk * 64 + h * 8); acc[1] = f32x16{}; };
        if (nb == 2) proj_pass<2, false, false>(lds, g, woff, acc, init2, pubq);
        else if (c == 1) proj_pass<1, false, true>(lds, g, woff, acc, init1, pubq);
        else proj_pass<1, false, false>(lds, g, woff, acc, init1, pubq);
        LGKM_BAR();
        if (qpend) pubq_now();
        STAMP();
        const unsigned oh = OFF_H + (((tok0 >> 5) * 112 + (hl0 + blk) * 2) * 64 + lane) * 16;
        { const f32x16 v = mish16(acc[0]); WS_STORE(pk8(v, 0), oh); WS_STORE(pk8(v, 1), oh + 1024); }
        if (nb == 2) { const f32x16 v = mish16(acc[1]); WS_STORE(pk8(v, 0), oh + 2048); WS_STORE(pk8(v, 1), oh + 3072); }
        else if (c == 1) {
            const float e = __builtin_amdgcn_exp2f(acc[1][0] * -LOG2E);
            if (h == 0) __hip_atomic_store((GAS float*)(ws + OFF_G) + tok0 + l31, __builtin_amdgcn_rcpf(1.0f + e), __ATOMIC_RELAXED, __HIP_MEMORY_SCOPE_AGENT);
        }
        if (bid < 224) {
            const bf16x8 f = trd2(lds + L_W2 + (16 * wm + 4 * h + g.q) * 128 + g.tcol + (g.sw ^ (half * 64)), 1024);
            WS_STORE(__builtin_bit_cast(u32x4, f), OFF_W2F + ((((bid & 7) * 2 + half) * 112 + (bid >> 3) * 4 + wm) * 64 + lane) * 16);
        }
    }
    STAMP();
    bool hpend = true;
    auto pubh_now = [&] {
        VMW(0);
        if (lds_inc(sy, S_CNTH) == 7) {
            flag_set(ws, OFF_FH + (rb * 16 + c) * 8, tag);
            if (bid < 224) flag_set(ws, OFF_FW + ((bid & 7) * 32 + (bid >> 3)) * 8, tag);
        }
        hpend = false;
    };
    auto pubh = [&] { if (UNLIKELY(hpend && vm_idle())) pubh_now(); };
    while (min4(sy, S_KVM) < 1 + half) { pubh(); __builtin_amdgcn_s_sleep(1); }
    asm volatile("" ::: "memory");
    STAMP();
    const bf16x8 qA = ldsr128(lds + L_X + wm * 2048 + l31 * 32 + h * 16), qB = ldsr128(lds + L_X + wm * 2048 + 1024 + l31 * 32 + h * 16);
    const lds_cptr kp = lds + L_KV + half * 65536 + l31 * 32 + h * 16;
    const bool isone = (lane & 16) != 0;
    lds_cptr vp = lds + (isone ? L_ONES : L_KV + half * 65536 + 32768) + h * 256 + (lane & 15) * 16;
    const int vstep = isone ? 0 : 4096;
    f32x16 OA = {}, OB = {}, SA, SB;
    const f32x16 Z = {};
    bf16x8 kf = ldsr128(kp), v0 = ldsr128(vp), v1 = ldsr128(vp + 512);
    u32x4 p0, p1 = {0, 0, 0, 0};
    SA = MFMA(kf, qA, Z);
#define EX(i) S_[i] = __builtin_amdgcn_exp2f(S_[i])
#define UNIT(S, SN, QN, O, OP, LD0, LD1, LD2)                                                       \
    {                                                                                               \
        f32x16& S_ = S;                                                                             \
        SN = MFMA(kf, QN, Z); PIN(SN); SBAR();                                                      \
        LD0; EX(0); EX(1); PIN(S_); SBAR();                                                         \
        OP = MFMA(v1, BC8(p1), OP); PIN(OP); SBAR();                                                \
        LD1; EX(2); EX(3); EX(4); EX(5); EX(6); EX(7); PIN(S_); SBAR();                             \
        p0 = pk8(S_, 0); PIN(p0); SBAR();                                                           \
        EX(8); EX(9); PIN(S_); SBAR();                                                              \
        O = MFMA(v0, BC8(p0), O); PIN(O); SBAR();                                                   \
        LD2; EX(10); EX(11); EX(12); EX(13); EX(14); EX(15); PIN(S_); SBAR();                       \
        p1 = pk8(S_, 1); PIN(p1); SBAR();                                                           \
    }
#define TILE(t, u)                                                                                  \
    UNIT(SA, SB, qB, OA, OB, kf = ldsr128(kp + (((t) + 1) & 31) * 1024), v1 = ldsr128(vp + (u) * 1024 + 512), (void)0) \
    UNIT(SB, SA, qA, OB, OA, (void)0, (void)0, v0 = ldsr128((u) == 3 ? vp + vstep : vp + ((u) + 1) * 1024))
    __builtin_amdgcn_s_setprio(2);
#pragma unroll 1
    for (int t = 0; t < 32; t += 4) {
        if (t == 4) { lds_add1(sy, S_QRD); spin4(sy, S_KVM, 3 + half); }
        if (t == 12) __builtin_amdgcn_s_setprio(1);
        if (t == 16) lds_add1(sy, S_P16);
        if (t == 24) { __builtin_amdgcn_s_setprio(0); lds_add1(sy, S_P24); spin1(sy, S_QRD, 8); }
        TILE(t, 0) pubh(); TILE(t + 1, 1) pubh(); TILE(t + 2, 2) pubh(); TILE(t + 3, 3) pubh();
        vp += vstep;
    }
    if (hpend) pubh_now();
    OB = MFMA(v1, BC8(p1), OB);
    PIN(kf); PIN(v0);
    LGW();
    lds_add1(sy, S_DONE);
    STAMP();
    {
        const f32x16 give = half ? OA : OB;
        LAS f32x4* dst = (LAS f32x4*)(lds + L_X + w * 2048) + lane;
        dst[0] = f32x4{give[0], give[1], give[2], give[3]};
        dst[64] = f32x4{give[4], give[5], give[6], give[7]};
        ((LAS float*)(lds + L_GATE + w * 128))[l31] = give[8];
        LGW(); sig(sy, S_FX + w * 4, 1);
    }
    f32x16 O = half ? OB : OA;
    spin1(sy, S_FX + (w ^ 4) * 4, 1);
    {
        LAS const f32x4* src = (LAS const f32x4*)(lds + L_X + (w ^ 4) * 2048) + lane;
        const f32x4 a = src[0], b = src[64];
#pragma unroll
        for (int r = 0; r < 4; ++r) { O[r] += a[r]; O[4 + r] += b[r]; }
        O[8] += ((LAS const float*)(lds + L_GATE + (w ^ 4) * 128))[l31];
    }
    bool bad = !(O[8] > 0x1p-90f && O[8] < 0x1p100f);
#pragma unroll
    for (int r = 0; r < 8; ++r) bad |= !(__builtin_fabsf(O[r]) < 0x1p120f);
    if (__any(bad)) {
        const bf16x8 qf = half ? qB : qA;
        const unsigned o1 = (lane & 11) == 0 ? 0x3f803f80u : 0u;
        float m = -INFINITY;
        O = Z;
#pragma unroll 1
        for (int t = 0; t < 64; ++t) {
            f32x16 s = MFMA(BC8(WS_LOAD(l31 * 32 + h * 16, OFF_K + head * 65536 + t * 1024)), qf, Z);
            u32x4 va = WS_LOAD(h * 256 + (lane & 15) * 16, OFF_V + head * 65536 + t * 1024);
            u32x4 vb = WS_LOAD(h * 256 + (lane & 15) * 16, OFF_V + head * 65536 + t * 1024 + 512);
            if (isone) { va = u32x4{o1, o1, o1, o1}; vb = va; }
            float tm = s[0];
#pragma unroll
            for (int r = 1; r < 16; ++r) tm = __builtin_fmaxf(tm, s[r]);
            tm = __builtin_fmaxf(tm, __shfl_xor(tm, 32));
            const float mn = __builtin_fmaxf(m, tm);
            const float al = __builtin_amdgcn_exp2f(m - mn);
            m = mn;
#pragma unroll
            for (int r = 0; r < 16; ++r) { O[r] *= al; s[r] = __builtin_amdgcn_exp2f(s[r] - mn); }
            O = MFMA(BC8(va), BC8(pk8(s, 0)), O);
            O = MFMA(BC8(vb), BC8(pk8(s, 1)), O);
        }
    }
    {
        u32x4 ov = pk8(O * __builtin_amdgcn_rcpf(O[8]), 0);
#pragma unroll
        for (int i = 0; i < 4; ++i) ov[i] = ov[i] == POISON ? 0x7fff7fffu : ov[i];
        WS_STORE(ov, OFF_O + (((qblk * 8 + wm * 2 + half) * 32 + head) * 64 + lane) * 16);
    }
    sig(sy, S_FM + w * 4, 1);
    STAMP();
    const int wn = (w >> 1) & 1, wmm = w & 1;
    const int row = mt * 64 + wmm * 32 + l31;
    float gv = 0.f;
    spin4(sy, S_FB, 1);
    spin1(sy, S_DONE, 8);
    if (w < 4) gv = __hip_atomic_load((GAS float*)(ws + OFF_G) + row, __ATOMIC_RELAXED, __HIP_MEMORY_SCOPE_AGENT);
    const char* og = ws + OFF_O + (size_t)mt * 65536;
    {
        const int kq = ((w >> 1) + nt) & 3;
        const char* src = og + wmm * 32768 + kq * 8192;
        const unsigned dst = lds0 + L_KV + 16384 + kq * 32768 + wmm * 8192;
        glds4c(src, lane * 16, dst); glds4c(src + 4096, lane * 16, dst + 4096);
    }
    STAMP();
    VMW(0); sig(sy, (w < 4 ? S_OF : S_OF2 - 16) + w * 4, 1);
    spin4(sy, S_OF, 1); spin4(sy, S_OF2, 1);
    STAMP();
    const lds_cptr wa = lds + L_KV + half * 65536 + (4 * h + g.q) * 128 + (g.sw ^ (wn * 64)) + g.tcol;
    const lds_cptr oa = lds + L_KV + 16384 + half * 65536 + wmm * 8192 + lane * 16;
    f32x16 acc = {};
    int budget = 1 << 18;
#pragma unroll
    for (int q4 = 0; q4 < 4; ++q4) {
        u32x4 of[4];
        bf16x8 wv[4];
#pragma unroll
        for (int i = 0; i < 4; ++i) {
            of[i] = *(LAS const u32x4*)(oa + (q4 >> 1) * 32768 + ((q4 & 1) * 4 + i) * 1024);
            wv[i] = trd2(wa + (q4 >> 1) * 32768 + ((q4 & 1) * 4 + i) * 2048, 1024);
        }
        for (;;) {
            unsigned mx = 0;
#pragma unroll
            for (int i = 0; i < 4; ++i) mx = max(max(mx, max(of[i][0], of[i][1])), max(of[i][2], of[i][3]));
            if (!UNLIKELY(__any(mx == POISON)) || --budget < 0) break;
            __builtin_amdgcn_s_sleep(POLL_SLEEP);
            glds4c(og + wmm * 32768 + (half * 16 + q4 * 4) * 1024, lane * 16, lds0 + L_KV + 16384 + (half * 2 + (q4 >> 1)) * 32768 + wmm * 8192 + (q4 & 1) * 4096);
            VMW(0);
#pragma unroll
            for (int i = 0; i < 4; ++i) of[i] = *(LAS const u32x4*)(oa + (q4 >> 1) * 32768 + ((q4 & 1) * 4 + i) * 1024);
        }
#pragma unroll
        for (int i = 0; i < 4; ++i) acc = MFMA(wv[i], BC8(of[i]), acc);
    }
    STAMP();
    LAS f32x4* ex = (LAS f32x4*)(lds + L_X + wm * 4096) + lane;
    if (w >= 4) {
        spin4(sy, S_FM, 1); spin4(sy, S_FM + 16, 1);
#pragma unroll
        for (int i = 0; i < 4; ++i) ex[i * 64] = f32x4{acc[4 * i], acc[4 * i + 1], acc[4 * i + 2], acc[4 * i + 3]};
        LGW(); sig(sy, S_FY + wm * 4, 1);
    } else {
        spin1(sy, S_FY + wm * 4, 1);
#pragma unroll
        for (int i = 0; i < 4; ++i) {
            const f32x4 r = ex[i * 64];
#pragma unroll
            for (int e = 0; e < 4; ++e) acc[4 * i + e] += r[e];
        }
        f32x16 bq;
        {
            LAS const u32x4* pb = (LAS const u32x4*)(lds + L_W2 + wm * 2048) + lane;
            const u32x4 b0 = pb[0], b1v = pb[64];
#pragma unroll
            for (int i = 0; i < 4; ++i) { bq[2 * i] = bflo(b0[i]); bq[2 * i + 1] = bfhi(b0[i]); bq[8 + 2 * i] = bflo(b1v[i]); bq[8 + 2 * i + 1] = bfhi(b1v[i]); }
        }
        const f32x16 res = acc * gv + bq * (1.0f - gv);
        const u32x4 lo = pk8(res, 0), hi = pk8(res, 1);
#pragma unroll
        for (int k = 0; k < 2; ++k) {
            const u32x4 o = k ? hi : lo;
            auto r0 = __builtin_amdgcn_permlane32_swap(o[0], o[2], false, false);
            auto r1 = __builtin_amdgcn_permlane32_swap(o[1], o[3], false, false);
            *(GAS u32x4*)(out + (size_t)row * 1024 + (nt * 64 + wn * 32 + 16 * k + 8 * h) * 2) = u32x4{r0[0], r1[0], r0[1], r1[1]};
        }
    }
    STAMP(); STAMP_RT(); STAMP_OUT(0, ws);
    asm volatile(".Lcode_end_b:");
}

extern "C" void kernel_launch(void* const* d_in, const int* in_sizes, int n_in, void* d_out, int out_size, void* d_ws, size_t ws_size,
                              hipStream_t stream) {
    fused_block<<<256, 768, 0, stream>>>((const char*)d_in[0], (const char*)d_in[1], (const char*)d_in[2], (const char*)d_in[4],
                                         (const char*)d_in[5], (const char*)d_in[6], (const char*)d_in[7], (char*)d_ws, (char*)d_out);
}
```

```cpp
#include <hip/hip_runtime.h>
#include <hip/hip_bf16.h>
#include <stdint.h>
#define DGB 100

typedef short bf16x8 __attribute__((ext_vector_type(8)));
typedef short s16x4 __attribute__((ext_vector_type(4)));
typedef float f32x16 __attribute__((ext_vector_type(16)));
typedef float f32x2 __attribute__((ext_vector_type(2)));
typedef float f32x4 __attribute__((ext_vector_type(4)));
typedef __bf16 bf16x2_t __attribute__((ext_vector_type(2)));
typedef unsigned u32x4 __attribute__((ext_vector_type(4)));
typedef unsigned u32x2 __attribute__((ext_vector_type(2)));
typedef int i32x4 __attribute__((ext_vector_type(4)));
typedef unsigned long long u64;
#define LAS __attribute__((address_space(3)))
#define GAS __attribute__((address_space(1)))
typedef LAS const char* lds_cptr;
typedef LAS char* lds_ptr;
#define DI __device__ __forceinline__
#define MFMA(a, b, c) __builtin_amdgcn_mfma_f32_32x32x16_bf16((a), (b), (c), 0, 0, 0)
#define SBAR() __builtin_amdgcn_sched_barrier(0)
#define PIN(x) asm volatile("" : "+v"(x))
#define VMW(N) asm volatile("s_waitcnt vmcnt(%0)" ::"n"(N) : "memory")
#define LGKM_BAR() asm volatile("s_waitcnt lgkmcnt(0)\n\ts_barrier" ::: "memory")
#define VM_BAR(N) asm volatile("s_waitcnt vmcnt(%0)\n\ts_barrier" ::"n"(N) : "memory")
#define LGW() asm volatile("s_waitcnt lgkmcnt(0)" ::: "memory")
#define BC8(x) __builtin_bit_cast(bf16x8, (x))
#define RFL(x) __builtin_amdgcn_readfirstlane(x)

constexpr int SEQ = 2048;
constexpr unsigned MB = 1024 * 1024;
constexpr unsigned OFF_Q = 0, OFF_K = 2 * MB, OFF_V = 4 * MB, OFF_H = 6 * MB, OFF_W2F = 14 * MB, OFF_O = 16 * MB, OFF_G = 18 * MB,
                   OFF_FQ = 18 * MB + 16384, OFF_FH = OFF_FQ + 2048, OFF_FO = OFF_FH + 2048, OFF_FW = OFF_FO + 2048, OFF_EP = OFF_FW + 2048,
                   WS_USED = 19 * MB;
constexpr unsigned POISON = 0xffffffffu;
constexpr float LOG2E = 1.4426950408889634f;
constexpr float QSCALE = 0.25f * LOG2E;

constexpr int L_KV = 0;
constexpr int L_RING = 0, NSLOT = 8;
constexpr int L_X = 131072;
constexpr int L_W2 = 147456, L_GATE = 155648, L_ONES = 156672, L_SYNC = 160768, L_TOTAL = L_SYNC + 512;
constexpr int L_BIAS = L_SYNC + 256;
constexpr int S_QRD = 0, S_P24 = 4, S_EX = 16,  S_KVM = 48, S_QOK = 80, S_CNTQ = 84, S_CNTH = 88, S_DONE = 96, S_P16 = 100,
              S_HOK = 104, S_FX = 112  , S_FB = 144, S_WOF = 160, S_FY = 192, S_FM = 208  , S_OF = 64  , S_OF2 = 240  ;

#define TRACE(kid, i)
#define STAMP_DECL
#define STAMP()
#define STAMP1()
#define STAMP_RT()
#define STAMP_OUT(kid, wsp)

DI unsigned pk2(float a, float b) {
    f32x2 v = {a, b};
    return __builtin_bit_cast(unsigned, __builtin_convertvector(v, bf16x2_t));
}
DI u32x4 pk8(const f32x16& x, int s) {
    u32x4 r = {pk2(x[8 * s], x[8 * s + 1]), pk2(x[8 * s + 2], x[8 * s + 3]), pk2(x[8 * s + 4], x[8 * s + 5]), pk2(x[8 * s + 6], x[8 * s + 7])};
    return r;
}
DI float bflo(unsigned u) { return __uint_as_float(u << 16); }
DI float bfhi(unsigned u) { return __uint_as_float(u & 0xffff0000u); }
DI void glds16(const void* sbase, unsigned voff, unsigned lds_dst) {
    unsigned sv;
    asm volatile("s_mov_b32 %0, m0\n\ts_mov_b32 m0, %3\n\ts_nop 0\n\tglobal_load_lds_dwordx4 %1, %2\n\ts_mov_b32 m0, %0"
                 : "=&s"(sv) : "v"(voff), "s"(sbase), "s"(lds_dst) : "memory");
}
DI void glds1(const void* sbase, unsigned voff, unsigned lds_dst) {
    unsigned sv;
    asm volatile("s_mov_b32 %0, m0\n\ts_mov_b32 m0, %3\n\ts_nop 0\n\tglobal_load_lds_dword %1, %2\n\ts_mov_b32 m0, %0"
                 : "=&s"(sv) : "v"(voff), "s"(sbase), "s"(lds_dst) : "memory");
}
#define GLDS4_BODY(POL)                                                                                                    \
    unsigned sv;                                                                                                           \
    asm volatile("s_mov_b32 %0, m0\n\ts_mov_b32 m0, %6\n\ts_nop 0\n\tglobal_load_lds_dwordx4 %1, %5" POL                   \
                 "\n\tglobal_load_lds_dwordx4 %2, %5 offset:1024" POL "\n\tglobal_load_lds_dwordx4 %3, %5 offset:2048" POL \
                 "\n\tglobal_load_lds_dwordx4 %4, %5 offset:3072" POL "\n\ts_mov_b32 m0, %0"                               \
                 : "=&s"(sv) : "v"(v0), "v"(v1), "v"(v2), "v"(v3), "s"(sbase), "s"(lds_dst) : "memory")
DI void glds4(const void* sbase, unsigned v0, unsigned v1, unsigned v2, unsigned v3, unsigned lds_dst) { GLDS4_BODY(""); }
DI void glds4c(const void* sbase, unsigned v0, unsigned lds_dst) { const unsigned v1 = v0, v2 = v0, v3 = v0; GLDS4_BODY(" sc1"); }
DI bf16x8 ldsr128(lds_cptr p) { return *(LAS const bf16x8*)p; }
DI s16x4 trd(lds_cptr p) { return __builtin_amdgcn_ds_read_tr16_b64_v4i16((LAS s16x4*)p); }
DI bf16x8 trd2(lds_cptr p, int second) {
    s16x4 lo = trd(p), hi = trd(p + second);
    return __builtin_shufflevector(lo, hi, 0, 1, 2, 3, 4, 5, 6, 7);
}
DI f32x16 mish16(const f32x16& h) {
    f32x16 e = h * LOG2E;
#pragma unroll
    for (int i = 0; i < 16; ++i) e[i] = __builtin_amdgcn_exp2f(e[i]);
    const f32x16 mh = {-0.5f, -0.5f, -0.5f, -0.5f, -0.5f, -0.5f, -0.5f, -0.5f, -0.5f, -0.5f, -0.5f, -0.5f, -0.5f, -0.5f, -0.5f, -0.5f};
    const f32x16 m1 = mh + mh;
    f32x16 d = __builtin_elementwise_fma(e, __builtin_elementwise_fma(e, mh, m1), m1);
#pragma unroll
    for (int i = 0; i < 16; ++i) d[i] = __builtin_amdgcn_rcpf(d[i]);
    return __builtin_elementwise_fma(h, d, h);
}
DI bool vm_idle() {
    unsigned r;
    asm volatile("s_getreg_b32 %0, hwreg(HW_REG_IB_STS, 0, 4)" : "=s"(r)::"memory");
    return r == 0;
}
DI void sig(lds_ptr sy, int off, int v) {
    asm volatile("s_mov_b64 exec, 1\n\tds_write_b32 %0, %1\n\ts_mov_b64 exec, -1" ::"v"((unsigned)(uintptr_t)(sy + off)), "v"(v) : "memory");
}
#define UNLIKELY(x) __builtin_expect(!!(x), 0)
DI void spin1(lds_ptr sy, int off, int need) {
    while (RFL(*(LAS volatile int*)(sy + off)) < need) __builtin_amdgcn_s_sleep(1);
    asm volatile("" ::: "memory");
}
DI int min4(lds_ptr sy, int off) {
    const i32x4 v = *(LAS volatile i32x4*)(sy + off);
    return RFL(min(min(v[0], v[1]), min(v[2], v[3])));
}
DI int min8(lds_ptr sy, int off) {
    const i32x4 v = *(LAS volatile i32x4*)(sy + off), u = *(LAS volatile i32x4*)(sy + off + 16);
    return RFL(min(min(min(v[0], v[1]), min(v[2], v[3])), min(min(u[0], u[1]), min(u[2], u[3]))));
}
DI void spin4(lds_ptr sy, int off, int need) {
    while (min4(sy, off) < need) __builtin_amdgcn_s_sleep(1);
    asm volatile("" ::: "memory");
}
DI void spin4f(lds_ptr sy, int off, int need) {
    while (min4(sy, off) < need) {}
    asm volatile("" ::: "memory");
}
DI void lds_add1(lds_ptr sy, int off) {
    asm volatile("s_mov_b64 exec, 1\n\tds_add_u32 %0, %1\n\ts_mov_b64 exec, -1" ::"v"((unsigned)(uintptr_t)(sy + off)), "v"(1) : "memory");
}
DI int lds_inc(lds_ptr sy, int off) {
    int r = 0;
    if ((threadIdx.x & 63) == 0) r = __hip_atomic_fetch_add((LAS int*)(sy + off), 1, __ATOMIC_RELAXED, __HIP_MEMORY_SCOPE_WORKGROUP);
    return RFL(r);
}
#define POLL_SLEEP 2
DI void flag_set(char* ws, unsigned off, u64 tag) {
    if ((threadIdx.x & 63) == 0) __hip_atomic_store((GAS u64*)(ws + off), tag, __ATOMIC_RELAXED, __HIP_MEMORY_SCOPE_AGENT);
}
template <int N>
DI void flag_wait(char* ws, unsigned laneoff, u64 tag) {
    GAS u64* p = (GAS u64*)(ws + laneoff);
    for (int spins = 0; spins < (1 << 20); ++spins) {
        bool ok = true;
#pragma unroll
        for (int i = 0; i < N; ++i) ok &= __hip_atomic_load(p + i * 64, __ATOMIC_RELAXED, __HIP_MEMORY_SCOPE_AGENT) == tag;
        if (__all(ok)) break;
        __builtin_amdgcn_s_sleep(POLL_SLEEP);
    }
    asm volatile("" ::: "memory");
}
#define WS_STORE(v, off) __builtin_amdgcn_raw_buffer_store_b128((v), wsr, (off), 0, 16)
#define WS_LOAD(voff, soff) __builtin_amdgcn_raw_buffer_load_b128(wsr, (voff), (soff), 16)

struct Geo {
    int lane, w, wm, h, l31, rot, q, tcol, sw;
};

template <int NB, bool SWAP, bool GATE, class Init, class Hook>
DI void proj_pass(lds_ptr lds, const Geo& g, int woff, f32x16 (&acc)[2], Init init, Hook hook) {
    const int xr = g.wm * 32 + g.l31;
    int xo[4];
#pragma unroll
    for (int s = 0; s < 4; ++s) xo[s] = L_RING + 16384 + xr * 128 + (((2 * s + g.h) ^ ((xr >> 1) & 7)) << 4);
    const int w0 = (L_RING + (8 * g.h + g.q) * 128 + g.sw + g.tcol) ^ woff, w1 = w0 ^ 64;
    constexpr int NF = NB + (GATE ? 1 : 0);
    bf16x8 xf[4], wf[4][NF];
    auto rd = [&](int s, int b, int t) {
        const int so = (t & 3) * 32768;
        if (b == 0) xf[s] = ldsr128(lds + xo[s] + so);
        if (b < NB) wf[s][b] = trd2(lds + ((b ? w1 : w0) + so) + s * 2048, 512);
        else wf[s][b] = ldsr128(lds + L_GATE + ((t + g.rot) & 7) * 128 + g.h * 16 + s * 32);
        SBAR();
    };
    auto mm = [&](int s, int b) {
        acc[b] = SWAP ? MFMA(xf[s], wf[s][b], acc[b]) : MFMA(wf[s][b], xf[s], acc[b]);
        SBAR();
    };
    LGKM_BAR(); SBAR();
    init();
#pragma unroll
    for (int s = 0; s < 4; ++s)
#pragma unroll
        for (int b = 0; b < NF; ++b) { if (s >= 2) mm(s - 2, b); rd(s, b, 0); }
#pragma unroll 1
    for (int t = 1; t < 8; ++t) {
        hook();
        LGKM_BAR(); SBAR();
#pragma unroll
        for (int s = 0; s < 4; ++s)
#pragma unroll
            for (int b = 0; b < NF; ++b) { mm((s + 2) & 3, b); rd(s, b, t); }
    }
#pragma unroll
    for (int s = 2; s < 4; ++s)
#pragma unroll
        for (int b = 0; b < NF; ++b) mm(s, b);
}
DI f32x16 bias16(lds_cptr p) {
    f32x16 v;
#pragma unroll
    for (int i = 0; i < 4; ++i) {
        const u32x2 bb = *(LAS const u32x2*)(p + 16 * i);
        v[4 * i] = bflo(bb[0]); v[4 * i + 1] = bfhi(bb[0]); v[4 * i + 2] = bflo(bb[1]); v[4 * i + 3] = bfhi(bb[1]);
    }
    return v;
}

#define LOOKA 8
#define PACE 2

__global__ __launch_bounds__(768, 3) void fused_block(const char* __restrict__ x, const char* __restrict__ wqkv, const char* __restrict__ wo,
                                                      const char* __restrict__ w1, const char* __restrict__ b1, const char* __restrict__ w2,
                                                      const char* __restrict__ gate, char* __restrict__ ws, char* __restrict__ out) {
    __shared__ __attribute__((aligned(1024))) char ldsmem[L_TOTAL];
    const int tid = threadIdx.x, lane = tid & 63, w = RFL(tid >> 6), wm = w & 3, h = lane >> 5, l31 = lane & 31;
    const int bid = blockIdx.x, xcd = bid & 7, idx = bid >> 3;
    const unsigned lds0 = (unsigned)(uintptr_t)ldsmem;
    const lds_ptr lds = (lds_ptr)ldsmem;
    const lds_ptr sy = lds + L_SYNC;
    const u64 dg_t0 = __builtin_amdgcn_s_memrealtime(); const u64 dg_c0 = __builtin_amdgcn_s_memtime(); u64 dg_x = 0;
    u64 code_pc;
    unsigned code_len;
    asm volatile("s_getpc_b64 %0\n.Lcode_here:\n\ts_max_i32 %1, .Lcode_end_a-.Lcode_here, 0\n\ts_max_i32 %1, .Lcode_end_b-.Lcode_here, %1"
                 : "=s"(code_pc), "=&s"(code_len)::"scc");
    const int rb = (xcd & 3) * 4 + (idx & 3), c = (xcd >> 2) * 8 + (idx >> 2), j = c >> 1, odd = c & 1;
    const int rot = ((idx >> 2) + 2 * (idx & 3)) & 7;
    const int nh4 = ((j ^ c) & 1) == 0;
    const int hb0 = 7 * j + (odd ? (nh4 ? 3 : 4) : 0), hl0 = hb0 & ~1;
    const int head = xcd * 4 + (idx >> 3), qblk = idx & 7;
    const int mt = xcd * 4 + (idx >> 3), nt = idx & 7;
    u64 tag;
#define EPOCH_LOAD() asm volatile("s_load_dwordx2 %0, %1, 0x0" : "=&s"(tag) : "s"(ws + OFF_EP) : "memory")
#define EPOCH_WAIT() do { asm volatile("s_waitcnt lgkmcnt(0)" : "+s"(tag)::"memory"); tag += 1; } while (0)
    const __amdgpu_buffer_rsrc_t wsr = __builtin_amdgcn_make_buffer_rsrc(ws, 0, WS_USED, 0x00020000);
    const int wch = ((lane & 7) ^ (((lane >> 4) & 1) << 2)) * 16;
    const char* kg = ws + OFF_K + (size_t)head * 65536;
    const char* vg = ws + OFF_V + (size_t)head * 65536;

    if (__builtin_expect(w >= 8, 1)) {
        const int r8 = lane >> 3;
        const unsigned vx = (wm * 32 + r8) * 1024, cx0 = ((lane & 7) ^ (r8 >> 1)) << 4, cx1 = cx0 ^ 64;
        const unsigned vx0 = vx + cx0, vx1 = vx + 7168 + cx1, vx2 = vx + 2 * 7168 + cx0, vx3 = vx + 3 * 7168 + cx1;
        const unsigned vq = ((wm & 1) * 32 + r8) * 3072 + wch + (wm >> 1) * 1024, vh = ((wm & 1) * 32 + r8) * 3584 + wch + (wm >> 1) * 128;
        const char* xb = x + (size_t)rb * 128 * 1024;
        const char* qb = wqkv + j * 128 + odd * 1024;
        const char* hb = w1 + hl0 * 64;
        const unsigned rd0 = lds0 + L_RING + wm * 4096;
        auto unit = [&](int u) {
            const int ks = ((u >> 1) + rot) & 7;
            const unsigned d = rd0 + (u % NSLOT) * 16384;
            if (u & 1) glds4(xb + ks * 128, vx0, vx1, vx2, vx3, d);
            else if (u < 16) glds4(qb + ks * (64 * 3072), vq, vq + (8 * 3072 - 1024), vq + 2 * (8 * 3072 - 1024), vq + 3 * (8 * 3072 - 1024), d);
            else glds4(hb + ks * (64 * 3584), vh, vh + (8 * 3584 - 1024), vh + 2 * (8 * 3584 - 1024), vh + 3 * (8 * 3584 - 1024), d);
        };
        { const unsigned o1 = (lane & 11) == 0 ? 0x3f803f80u : 0u; *(LAS u32x4*)(lds + L_ONES + (tid - 512) * 16) = u32x4{o1, o1, o1, o1}; }
        if (w == 8) *(LAS int*)(sy + lane * 4) = 0;
        __builtin_amdgcn_s_setprio(1);
        unit(0); unit(1);
        EPOCH_LOAD();
        unit(2);
        EPOCH_WAIT();
        STAMP();
        VM_BAR(4);
        STAMP();
        unit(3); unit(4); unit(5); unit(6); unit(7);
        unsigned pf;
        {
            const int pt = bid < 224 ? bid : 223;
            const char* s2 = w2 + (size_t)(pt >> 3) * 65536 + (pt & 7) * 128;
            const unsigned v2 = (wm * 16 + r8) * 1024 + wch;
            glds16(s2, v2, lds0 + L_W2 + wm * 2048); glds16(s2 + 8192, v2, lds0 + L_W2 + wm * 2048 + 1024);
            if (wm & 1) glds1(b1 + hl0 * 64, lane * 4, lds0 + L_BIAS); else glds16(gate, lane * 16, lds0 + L_GATE);
            asm volatile("global_load_dword %0, %1, %2" : "=v"(pf) : "v"(((idx & 3) * 128 + wm * 32 + l31) * 3584 + h * 128), "s"(hb) : "memory");
        }
#pragma unroll
        for (int t = 1; t < 16; ++t) {
            if (t < 4) VM_BAR(20); else if (t < 14) VM_BAR(16); else if (t == 14) VM_BAR(8); else VM_BAR(0);
            if (2 * t + 6 < 32) { unit(2 * t + 6); unit(2 * t + 7); }
            if (t == 4 || t == 8 || t == 12) STAMP();
        }
        PIN(pf);
        STAMP();
        if (w == 8) { flag_wait<1>(ws, OFF_FQ + (head >> 2) * 256 + l31 * 8, tag); sig(sy, S_QOK, 1); }
        const char* kvg = (wm & 1) ? vg : kg;
        const unsigned kvd = lds0 + L_KV + (wm & 1) * 32768 + (wm >> 1) * 4096;
        asm volatile("s_barrier" ::: "memory");
        spin1(sy, S_QOK, 1);
        if (wm < 2) glds4c(ws + OFF_Q + (size_t)(head * SEQ + qblk * 256) * 32 + wm * 4096, lane * 16, lds0 + L_X + wm * 4096);
#pragma unroll
        for (int i = 0; i < 8; ++i) {
            const int hf = i < 2 ? i : (i < 5 ? 0 : 1), gi = i < 2 ? 0 : (i < 5 ? i - 1 : i - 4);
            glds4c(kvg + hf * 32768 + (((wm >> 1) + 2 * gi + qblk) & 7) * 4096, lane * 16, kvd + hf * 65536 + gi * 8192);
            if (i == 2) { VMW(8); sig(sy, S_KVM + wm * 4, 1); }
            if (i == 3) { VMW(8); sig(sy, S_KVM + wm * 4, 2); }
            if (i == 6) { VMW(8); sig(sy, S_KVM + wm * 4, 3); }
        }
        VMW(0); sig(sy, S_KVM + wm * 4, 4);
        STAMP();
        __builtin_amdgcn_s_setprio(3);
        if (w == 8) {
            flag_wait<1>(ws, h ? OFF_FH + ((mt >> 1) * 16 + (lane & 15)) * 8 : OFF_FW + (nt * 32 + (l31 < 28 ? l31 : 0)) * 8, tag);
            sig(sy, S_HOK, 1);
        } else spin1(sy, S_HOK, 1);
        STAMP();
        constexpr int PD = 4;
        const int hs = OFF_H + ((mt * 2 + (wm & 1)) * 112 + wm * 28) * 1024, hs1 = OFF_H + ((mt * 2 + ((wm & 1) ^ 1)) * 112 + wm * 28) * 1024;
        const int wso = OFF_W2F + ((nt * 2 + (wm >> 1)) * 112 + wm * 28) * 1024, wso1 = OFF_W2F + ((nt * 2 + ((wm >> 1) ^ 1)) * 112 + wm * 28) * 1024;
        u32x4 hf[PD][2], wf[PD][2];
        auto ld = [&](int i, int k) {
            hf[i][0] = WS_LOAD(lane * 16, hs + k * 1024); wf[i][0] = WS_LOAD(lane * 16, wso + k * 1024);
            hf[i][1] = WS_LOAD(lane * 16, hs1 + k * 1024); wf[i][1] = WS_LOAD(lane * 16, wso1 + k * 1024);
            SBAR();
        };
#pragma unroll
        for (int i = 0; i < PD; ++i) ld(i, i);
        f32x16 acc[2][2] = {};
        bool wopend = true;
        auto load_wo = [&] {
            const char* wg = wo + nt * 128 + (size_t)wm * 131072;
            const unsigned vow = r8 * 1024 + wch;
#pragma unroll
            for (int i = 0; i < 4; ++i) glds4(wg + i * 32768, vow, vow + 7168, vow + 2 * 7168, vow + 3 * 7168, lds0 + L_KV + wm * 32768 + i * 4096);
            wopend = false;
        };
#pragma unroll 1
        for (int k0 = 0; k0 < 28; k0 += PD) {
#pragma unroll
            for (int i = 0; i < PD; ++i) {
                const u32x2 st2 = *(LAS volatile u32x2*)(sy + S_DONE);
                const bool slow = RFL(st2[0]) < 8;
                if (UNLIKELY(wopend && RFL(st2[1]) == 8)) load_wo();
#pragma unroll
                for (int n = 0; n < 2; ++n)
#pragma unroll
                    for (int m = 0; m < 2; ++m) {
                        acc[n][m] = MFMA(BC8(wf[i][n]), BC8(hf[i][m]), acc[n][m]);
                        SBAR(); if (slow) __builtin_amdgcn_s_sleep(PACE); SBAR();
                    }
                const int kn = k0 + PD + i;
                ld(i, kn < 28 ? kn : 27);
            }
        }
        STAMP();
        if (wopend) { spin1(sy, S_P16, 8); load_wo(); }
        VMW(0);
        spin1(sy, S_P24, 8);
        f32x16 keep = acc[0][0];
#pragma unroll
        for (int r = 1; r < 4; ++r) {
            const f32x16& gv = acc[r >> 1][r & 1];
            if (r == 3) spin1(sy, S_EX + (wm ^ 1) * 4, 2);
            LAS f32x4* dst = (LAS f32x4*)(lds + L_KV + wm * 32768 + 16384 + (r & 1) * 4096) + lane;
#pragma unroll
            for (int i = 0; i < 4; ++i) dst[i * 64] = f32x4{gv[4 * i], gv[4 * i + 1], gv[4 * i + 2], gv[4 * i + 3]};
            LGW(); sig(sy, S_EX + wm * 4, r);
            spin1(sy, S_EX + (wm ^ r) * 4, r);
            LAS const f32x4* src = (LAS const f32x4*)(lds + L_KV + (wm ^ r) * 32768 + 16384 + (r & 1) * 4096) + lane;
#pragma unroll
            for (int i = 0; i < 4; ++i) {
                const f32x4 v = src[i * 64];
#pragma unroll
                for (int e = 0; e < 4; ++e) keep[4 * i + e] += v[e];
            }
        }
        { LAS u32x4* pb = (LAS u32x4*)(lds + L_W2 + wm * 2048) + lane; pb[0] = pk8(keep, 0); pb[64] = pk8(keep, 1); }
        LGW(); sig(sy, S_FB + wm * 4, 1);
        if (bid == 0 && w == 8) {
            flag_wait<4>(ws, OFF_FH + lane * 8, tag);
            flag_set(ws, OFF_EP, tag);
        }
        STAMP(); STAMP_RT(); STAMP_OUT(0, ws);
        asm volatile(".Lcode_end_a:");
        return;
    }

    Geo g;
    g.lane = lane; g.w = w; g.wm = wm; g.h = h; g.l31 = l31; g.rot = rot;
    g.q = (lane & 15) >> 2; g.tcol = ((lane >> 4) & 1) * 32 + (lane & 3) * 8; g.sw = (g.q >> 1) << 6;
    const int tok0 = rb * 128 + wm * 32, half = w >> 2;
    unsigned cpf = 0;
    if (w == 0) {
        const unsigned o = (lane * 32 + idx) * 128;
        if (o < code_len) asm volatile("global_load_dword %0, %1, %2" : "=v"(cpf) : "v"(o), "s"(code_pc) : "memory");
    }
    EPOCH_LOAD();
    EPOCH_WAIT();
    __builtin_amdgcn_s_setprio(2);
    {
        f32x16 acc[2] = {};
        auto nohook = [] {};
        const unsigned o = (4 * j * SEQ + tok0 + l31) * 32 + h * 16;
        if (half) {
            proj_pass<1, false, false>(lds, g, odd ? 64 : 8192, acc, nohook, nohook);
            STAMP();
#pragma unroll
            for (int s = 0; s < 2; ++s) WS_STORE(pk8(acc[0], s), OFF_K + o + (2 * odd + s) * SEQ * 32);
        } else if (!odd) {
            proj_pass<2, false, false>(lds, g, 0, acc, nohook, nohook);
            STAMP();
#pragma unroll
            for (int i = 0; i < 4; ++i) WS_STORE(pk8(acc[i >> 1] * QSCALE, i & 1), OFF_Q + o + i * SEQ * 32);
        } else {
            proj_pass<2, true, false>(lds, g, 8192, acc, nohook, nohook);
            STAMP();
            const unsigned ov = OFF_V + ((4 * j + (l31 >> 4)) * 128 + (tok0 >> 4)) * 512 + h * 256 + (lane & 15) * 16;
#pragma unroll
            for (int i = 0; i < 4; ++i) WS_STORE(pk8(acc[i >> 1], i & 1), ov + (i >> 1) * 2 * 65536 + (i & 1) * 512);
        }
    }
    dg_x = __builtin_amdgcn_s_memrealtime() - dg_t0;
    WS_STORE((u32x4{POISON, POISON, POISON, POISON}), OFF_O + (((rb * 4 + wm) * 32 + 4 * j + 2 * odd + half) * 64 + lane) * 16);
    STAMP();
    {
        bool qpend = true;
        auto pubq_now = [&] {
            VMW(0); PIN(cpf);
            if (lds_inc(sy, S_CNTQ) == 7) flag_set(ws, OFF_FQ + ((j * 16 + rb) * 2 + odd) * 8, tag);
            qpend = false;
        };
        auto pubq = [&] { if (UNLIKELY(qpend && vm_idle())) pubq_now(); };
        const int pB = (nh4 || (hb0 & 1)) ? 1 : 0;
        const int blk = half ? 2 * pB : (nh4 ? 0 : (pB ? 1 : 2));
        const int nb = (half || nh4) ? 2 : 1;
        f32x16 acc[2];
        const int woff = (blk >> 1) * 8192 + (blk & 1) * 64;
        auto init2 = [&] { acc[0] = bias16(lds + L_BIAS + blk * 64 + h * 8); acc[1] = bias16(lds + L_BIAS + blk * 64 + 64 + h * 8); };
        auto init1 = [&] { acc[0] = bias16(lds + L_BIAS + blk * 64 + h * 8); acc[1] = f32x16{}; };
        if (nb == 2) proj_pass<2, false, false>(lds, g, woff, acc, init2, pubq);
        else if (c == 1) proj_pass<1, false, true>(lds, g, woff, acc, init1, pubq);
        else proj_pass<1, false, false>(lds, g, woff, acc, init1, pubq);
        LGKM_BAR();
        if (qpend) pubq_now();
        STAMP();
        const unsigned oh = OFF_H + (((tok0 >> 5) * 112 + (hl0 + blk) * 2) * 64 + lane) * 16;
        { const f32x16 v = mish16(acc[0]); WS_STORE(pk8(v, 0), oh); WS_STORE(pk8(v, 1), oh + 1024); }
        if (nb == 2) { const f32x16 v = mish16(acc[1]); WS_STORE(pk8(v, 0), oh + 2048); WS_STORE(pk8(v, 1), oh + 3072); }
        else if (c == 1) {
            const float e = __builtin_amdgcn_exp2f(acc[1][0] * -LOG2E);
            if (h == 0) __hip_atomic_store((GAS float*)(ws + OFF_G) + tok0 + l31, __builtin_amdgcn_rcpf(1.0f + e), __ATOMIC_RELAXED, __HIP_MEMORY_SCOPE_AGENT);
        }
        if (bid < 224) {
            const bf16x8 f = trd2(lds + L_W2 + (16 * wm + 4 * h + g.q) * 128 + g.tcol + (g.sw ^ (half * 64)), 1024);
            WS_STORE(__builtin_bit_cast(u32x4, f), OFF_W2F + ((((bid & 7) * 2 + half) * 112 + (bid >> 3) * 4 + wm) * 64 + lane) * 16);
        }
    }
    STAMP();
    bool hpend = true;
    auto pubh_now = [&] {
        VMW(0);
        if (lds_inc(sy, S_CNTH) == 7) {
            flag_set(ws, OFF_FH + (rb * 16 + c) * 8, tag);
            if (bid < 224) flag_set(ws, OFF_FW + ((bid & 7) * 32 + (bid >> 3)) * 8, tag);
        }
        hpend = false;
    };
    auto pubh = [&] { if (UNLIKELY(hpend && vm_idle())) pubh_now(); };
    while (min4(sy, S_KVM) < 1 + half) { pubh(); __builtin_amdgcn_s_sleep(1); }
    asm volatile("" ::: "memory");
    STAMP();
    const bf16x8 qA = ldsr128(lds + L_X + wm * 2048 + l31 * 32 + h * 16), qB = ldsr128(lds + L_X + wm * 2048 + 1024 + l31 * 32 + h * 16);
    const lds_cptr kp = lds + L_KV + half * 65536 + l31 * 32 + h * 16;
    const bool isone = (lane & 16) != 0;
    lds_cptr vp = lds + (isone ? L_ONES : L_KV + half * 65536 + 32768) + h * 256 + (lane & 15) * 16;
    const int vstep = isone ? 0 : 4096;
    f32x16 OA = {}, OB = {}, SA, SB;
    const f32x16 Z = {};
    bf16x8 kf = ldsr128(kp), v0 = ldsr128(vp), v1 = ldsr128(vp + 512);
    u32x4 p0, p1 = {0, 0, 0, 0};
    SA = MFMA(kf, qA, Z);
#define EX(i) S_[i] = __builtin_amdgcn_exp2f(S_[i])
#define UNIT(S, SN, QN, O, OP, LD0, LD1, LD2)                                                       \
    {                                                                                               \
        f32x16& S_ = S;                                                                             \
        SN = MFMA(kf, QN, Z); PIN(SN); SBAR();                                                      \
        LD0; EX(0); EX(1); PIN(S_); SBAR();                                                         \
        OP = MFMA(v1, BC8(p1), OP); PIN(OP); SBAR();                                                \
        LD1; EX(2); EX(3); EX(4); EX(5); EX(6); EX(7); PIN(S_); SBAR();                             \
        p0 = pk8(S_, 0); PIN(p0); SBAR();                                                           \
        EX(8); EX(9); PIN(S_); SBAR();                                                              \
        O = MFMA(v0, BC8(p0), O); PIN(O); SBAR();                                                   \
        LD2; EX(10); EX(11); EX(12); EX(13); EX(14); EX(15); PIN(S_); SBAR();                       \
        p1 = pk8(S_, 1); PIN(p1); SBAR();                                                           \
    }
#define TILE(t, u)                                                                                  \
    UNIT(SA, SB, qB, OA, OB, kf = ldsr128(kp + (((t) + 1) & 31) * 1024), v1 = ldsr128(vp + (u) * 1024 + 512), (void)0) \
    UNIT(SB, SA, qA, OB, OA, (void)0, (void)0, v0 = ldsr128((u) == 3 ? vp + vstep : vp + ((u) + 1) * 1024))
    __builtin_amdgcn_s_setprio(2);
#pragma unroll 1
    for (int t = 0; t < 32; t += 4) {
        if (t == 4) { lds_add1(sy, S_QRD); spin4(sy, S_KVM, 3 + half); }
        if (t == 12) __builtin_amdgcn_s_setprio(1);
        if (t == 16) lds_add1(sy, S_P16);
        if (t == 24) { __builtin_amdgcn_s_setprio(0); lds_add1(sy, S_P24); spin1(sy, S_QRD, 8); }
        TILE(t, 0) pubh(); TILE(t + 1, 1) pubh(); TILE(t + 2, 2) pubh(); TILE(t + 3, 3) pubh();
        vp += vstep;
    }
    if (hpend) pubh_now();
    OB = MFMA(v1, BC8(p1), OB);
    PIN(kf); PIN(v0);
    LGW();
    lds_add1(sy, S_DONE);
    STAMP();
    {
        const f32x16 give = half ? OA : OB;
        LAS f32x4* dst = (LAS f32x4*)(lds + L_X + w * 2048) + lane;
        dst[0] = f32x4{give[0], give[1], give[2], give[3]};
        dst[64] = f32x4{give[4], give[5], give[6], give[7]};
        ((LAS float*)(lds + L_GATE + w * 128))[l31] = give[8];
        LGW(); sig(sy, S_FX + w * 4, 1);
    }
    f32x16 O = half ? OB : OA;
    spin1(sy, S_FX + (w ^ 4) * 4, 1);
    {
        LAS const f32x4* src = (LAS const f32x4*)(lds + L_X + (w ^ 4) * 2048) + lane;
        const f32x4 a = src[0], b = src[64];
#pragma unroll
        for (int r = 0; r < 4; ++r) { O[r] += a[r]; O[4 + r] += b[r]; }
        O[8] += ((LAS const float*)(lds + L_GATE + (w ^ 4) * 128))[l31];
    }
    bool bad = !(O[8] > 0x1p-90f && O[8] < 0x1p100f);
#pragma unroll
    for (int r = 0; r < 8; ++r) bad |= !(__builtin_fabsf(O[r]) < 0x1p120f);
    if (__any(bad)) {
        const bf16x8 qf = half ? qB : qA;
        const unsigned o1 = (lane & 11) == 0 ? 0x3f803f80u : 0u;
        float m = -INFINITY;
        O = Z;
#pragma unroll 1
        for (int t = 0; t < 64; ++t) {
            f32x16 s = MFMA(BC8(WS_LOAD(l31 * 32 + h * 16, OFF_K + head * 65536 + t * 1024)), qf, Z);
            u32x4 va = WS_LOAD(h * 256 + (lane & 15) * 16, OFF_V + head * 65536 + t * 1024);
            u32x4 vb = WS_LOAD(h * 256 + (lane & 15) * 16, OFF_V + head * 65536 + t * 1024 + 512);
            if (isone) { va = u32x4{o1, o1, o1, o1}; vb = va; }
            float tm = s[0];
#pragma unroll
            for (int r = 1; r < 16; ++r) tm = __builtin_fmaxf(tm, s[r]);
            tm = __builtin_fmaxf(tm, __shfl_xor(tm, 32));
            const float mn = __builtin_fmaxf(m, tm);
            const float al = __builtin_amdgcn_exp2f(m - mn);
            m = mn;
#pragma unroll
            for (int r = 0; r < 16; ++r) { O[r] *= al; s[r] = __builtin_amdgcn_exp2f(s[r] - mn); }
            O = MFMA(BC8(va), BC8(pk8(s, 0)), O);
            O = MFMA(BC8(vb), BC8(pk8(s, 1)), O);
        }
    }
    {
        u32x4 ov = pk8(O * __builtin_amdgcn_rcpf(O[8]), 0);
#pragma unroll
        for (int i = 0; i < 4; ++i) ov[i] = ov[i] == POISON ? 0x7fff7fffu : ov[i];
        WS_STORE(ov, OFF_O + (((qblk * 8 + wm * 2 + half) * 32 + head) * 64 + lane) * 16);
    }
    sig(sy, S_FM + w * 4, 1);
    STAMP();
    const int wn = (w >> 1) & 1, wmm = w & 1;
    const int row = mt * 64 + wmm * 32 + l31;
    float gv = 0.f;
    spin4(sy, S_FB, 1);
    spin1(sy, S_DONE, 8);
    if (w < 4) gv = __hip_atomic_load((GAS float*)(ws + OFF_G) + row, __ATOMIC_RELAXED, __HIP_MEMORY_SCOPE_AGENT);
    const char* og = ws + OFF_O + (size_t)mt * 65536;
    {
        const int kq = ((w >> 1) + nt) & 3;
        const char* src = og + wmm * 32768 + kq * 8192;
        const unsigned dst = lds0 + L_KV + 16384 + kq * 32768 + wmm * 8192;
        glds4c(src, lane * 16, dst); glds4c(src + 4096, lane * 16, dst + 4096);
    }
    STAMP();
    VMW(0); sig(sy, (w < 4 ? S_OF : S_OF2 - 16) + w * 4, 1);
    spin4(sy, S_OF, 1); spin4(sy, S_OF2, 1);
    STAMP();
    const lds_cptr wa = lds + L_KV + half * 65536 + (4 * h + g.q) * 128 + (g.sw ^ (wn * 64)) + g.tcol;
    const lds_cptr oa = lds + L_KV + 16384 + half * 65536 + wmm * 8192 + lane * 16;
    f32x16 acc = {};
    int budget = 1 << 18;
#pragma unroll
    for (int q4 = 0; q4 < 4; ++q4) {
        u32x4 of[4];
        bf16x8 wv[4];
#pragma unroll
        for (int i = 0; i < 4; ++i) {
            of[i] = *(LAS const u32x4*)(oa + (q4 >> 1) * 32768 + ((q4 & 1) * 4 + i) * 1024);
            wv[i] = trd2(wa + (q4 >> 1) * 32768 + ((q4 & 1) * 4 + i) * 2048, 1024);
        }
        for (;;) {
            unsigned mx = 0;
#pragma unroll
            for (int i = 0; i < 4; ++i) mx = max(max(mx, max(of[i][0], of[i][1])), max(of[i][2], of[i][3]));
            if (!UNLIKELY(__any(mx == POISON)) || --budget < 0) break;
            __builtin_amdgcn_s_sleep(POLL_SLEEP);
            glds4c(og + wmm * 32768 + (half * 16 + q4 * 4) * 1024, lane * 16, lds0 + L_KV + 16384 + (half * 2 + (q4 >> 1)) * 32768 + wmm * 8192 + (q4 & 1) * 4096);
            VMW(0);
#pragma unroll
            for (int i = 0; i < 4; ++i) of[i] = *(LAS const u32x4*)(oa + (q4 >> 1) * 32768 + ((q4 & 1) * 4 + i) * 1024);
        }
#pragma unroll
        for (int i = 0; i < 4; ++i) acc = MFMA(wv[i], BC8(of[i]), acc);
    }
    STAMP();
    LAS f32x4* ex = (LAS f32x4*)(lds + L_X + wm * 4096) + lane;
    if (w >= 4) {
        spin4(sy, S_FM, 1); spin4(sy, S_FM + 16, 1);
#pragma unroll
        for (int i = 0; i < 4; ++i) ex[i * 64] = f32x4{acc[4 * i], acc[4 * i + 1], acc[4 * i + 2], acc[4 * i + 3]};
        LGW(); sig(sy, S_FY + wm * 4, 1);
    } else {
        spin1(sy, S_FY + wm * 4, 1);
#pragma unroll
        for (int i = 0; i < 4; ++i) {
            const f32x4 r = ex[i * 64];
#pragma unroll
            for (int e = 0; e < 4; ++e) acc[4 * i + e] += r[e];
        }
        f32x16 bq;
        {
            LAS const u32x4* pb = (LAS const u32x4*)(lds + L_W2 + wm * 2048) + lane;
            const u32x4 b0 = pb[0], b1v = pb[64];
#pragma unroll
            for (int i = 0; i < 4; ++i) { bq[2 * i] = bflo(b0[i]); bq[2 * i + 1] = bfhi(b0[i]); bq[8 + 2 * i] = bflo(b1v[i]); bq[8 + 2 * i + 1] = bfhi(b1v[i]); }
        }
        const f32x16 res = acc * gv + bq * (1.0f - gv);
        const u32x4 lo = pk8(res, 0), hi = pk8(res, 1);
#pragma unroll
        for (int k = 0; k < 2; ++k) {
            const u32x4 o = k ? hi : lo;
            auto r0 = __builtin_amdgcn_permlane32_swap(o[0], o[2], false, false);
            auto r1 = __builtin_amdgcn_permlane32_swap(o[1], o[3], false, false);
            *(GAS u32x4*)(out + (size_t)row * 1024 + (nt * 64 + wn * 32 + 16 * k + 8 * h) * 2) = u32x4{r0[0], r1[0], r0[1], r1[1]};
        }
    }
    STAMP(); STAMP_RT(); STAMP_OUT(0, ws);
    if (bid == DGB && w == 4) { const u64 e_ = __builtin_amdgcn_s_memrealtime() + 4 * dg_x; while (__builtin_amdgcn_s_memrealtime() < e_) __builtin_amdgcn_s_sleep(4); }
    asm volatile(".Lcode_end_b:");
}

extern "C" void kernel_launch(void* const* d_in, const int* in_sizes, int n_in, void* d_out, int out_size, void* d_ws, size_t ws_size,
                              hipStream_t stream) {
    fused_block<<<256, 768, 0, stream>>>((const char*)d_in[0], (const char*)d_in[1], (const char*)d_in[2], (const char*)d_in[4],
                                         (const char*)d_in[5], (const char*)d_in[6], (const char*)d_in[7], (char*)d_ws, (char*)d_out);
}
```

```cpp
#include <hip/hip_runtime.h>
#include <hip/hip_bf16.h>
#include <stdint.h>
#define DGB 100

typedef short bf16x8 __attribute__((ext_vector_type(8)));
typedef short s16x4 __attribute__((ext_vector_type(4)));
typedef float f32x16 __attribute__((ext_vector_type(16)));
typedef float f32x2 __attribute__((ext_vector_type(2)));
typedef float f32x4 __attribute__((ext_vector_type(4)));
typedef __bf16 bf16x2_t __attribute__((ext_vector_type(2)));
typedef unsigned u32x4 __attribute__((ext_vector_type(4)));
typedef unsigned u32x2 __attribute__((ext_vector_type(2)));
typedef int i32x4 __attribute__((ext_vector_type(4)));
typedef unsigned long long u64;
#define LAS __attribute__((address_space(3)))
#define GAS __attribute__((address_space(1)))
typedef LAS const char* lds_cptr;
typedef LAS char* lds_ptr;
#define DI __device__ __forceinline__
#define MFMA(a, b, c) __builtin_amdgcn_mfma_f32_32x32x16_bf16((a), (b), (c), 0, 0, 0)
#define SBAR() __builtin_amdgcn_sched_barrier(0)
#define PIN(x) asm volatile("" : "+v"(x))
#define VMW(N) asm volatile("s_waitcnt vmcnt(%0)" ::"n"(N) : "memory")
#define LGKM_BAR() asm volatile("s_waitcnt lgkmcnt(0)\n\ts_barrier" ::: "memory")
#define VM_BAR(N) asm volatile("s_waitcnt vmcnt(%0)\n\ts_barrier" ::"n"(N) : "memory")
#define LGW() asm volatile("s_waitcnt lgkmcnt(0)" ::: "memory")
#define BC8(x) __builtin_bit_cast(bf16x8, (x))
#define RFL(x) __builtin_amdgcn_readfirstlane(x)

constexpr int SEQ = 2048;
constexpr unsigned MB = 1024 * 1024;
constexpr unsigned OFF_Q = 0, OFF_K = 2 * MB, OFF_V = 4 * MB, OFF_H = 6 * MB, OFF_W2F = 14 * MB, OFF_O = 16 * MB, OFF_G = 18 * MB,
                   OFF_FQ = 18 * MB + 16384, OFF_FH = OFF_FQ + 2048, OFF_FO = OFF_FH + 2048, OFF_FW = OFF_FO + 2048, OFF_EP = OFF_FW + 2048,
                   WS_USED = 19 * MB;
constexpr unsigned POISON = 0xffffffffu;
constexpr float LOG2E = 1.4426950408889634f;
constexpr float QSCALE = 0.25f * LOG2E;

constexpr int L_KV = 0;
constexpr int L_RING = 0, NSLOT = 8;
constexpr int L_X = 131072;
constexpr int L_W2 = 147456, L_GATE = 155648, L_ONES = 156672, L_SYNC = 160768, L_TOTAL = L_SYNC + 512;
constexpr int L_BIAS = L_SYNC + 256;
constexpr int S_QRD = 0, S_P24 = 4, S_EX = 16,  S_KVM = 48, S_QOK = 80, S_CNTQ = 84, S_CNTH = 88, S_DONE = 96, S_P16 = 100,
              S_HOK = 104, S_FX = 112  , S_FB = 144, S_WOF = 160, S_FY = 192, S_FM = 208  , S_OF = 64  , S_OF2 = 240  ;

#define TRACE(kid, i)
#define STAMP_DECL
#define STAMP()
#define STAMP1()
#define STAMP_RT()
#define STAMP_OUT(kid, wsp)

DI unsigned pk2(float a, float b) {
    f32x2 v = {a, b};
    return __builtin_bit_cast(unsigned, __builtin_convertvector(v, bf16x2_t));
}
DI u32x4 pk8(const f32x16& x, int s) {
    u32x4 r = {pk2(x[8 * s], x[8 * s + 1]), pk2(x[8 * s + 2], x[8 * s + 3]), pk2(x[8 * s + 4], x[8 * s + 5]), pk2(x[8 * s + 6], x[8 * s + 7])};
    return r;
}
DI float bflo(unsigned u) { return __uint_as_float(u << 16); }
DI float bfhi(unsigned u) { return __uint_as_float(u & 0xffff0000u); }
DI void glds16(const void* sbase, unsigned voff, unsigned lds_dst) {
    unsigned sv;
    asm volatile("s_mov_b32 %0, m0\n\ts_mov_b32 m0, %3\n\ts_nop 0\n\tglobal_load_lds_dwordx4 %1, %2\n\ts_mov_b32 m0, %0"
                 : "=&s"(sv) : "v"(voff), "s"(sbase), "s"(lds_dst) : "memory");
}
DI void glds1(const void* sbase, unsigned voff, unsigned lds_dst) {
    unsigned sv;
    asm volatile("s_mov_b32 %0, m0\n\ts_mov_b32 m0, %3\n\ts_nop 0\n\tglobal_load_lds_dword %1, %2\n\ts_mov_b32 m0, %0"
                 : "=&s"(sv) : "v"(voff), "s"(sbase), "s"(lds_dst) : "memory");
}
#define GLDS4_BODY(POL)                                                                                                    \
    unsigned sv;                                                                                                           \
    asm volatile("s_mov_b32 %0, m0\n\ts_mov_b32 m0, %6\n\ts_nop 0\n\tglobal_load_lds_dwordx4 %1, %5" POL                   \
                 "\n\tglobal_load_lds_dwordx4 %2, %5 offset:1024" POL "\n\tglobal_load_lds_dwordx4 %3, %5 offset:2048" POL \
                 "\n\tglobal_load_lds_dwordx4 %4, %5 offset:3072" POL "\n\ts_mov_b32 m0, %0"                               \
                 : "=&s"(sv) : "v"(v0), "v"(v1), "v"(v2), "v"(v3), "s"(sbase), "s"(lds_dst) : "memory")
DI void glds4(const void* sbase, unsigned v0, unsigned v1, unsigned v2, unsigned v3, unsigned lds_dst) { GLDS4_BODY(""); }
DI void glds4c(const void* sbase, unsigned v0, unsigned lds_dst) { const unsigned v1 = v0, v2 = v0, v3 = v0; GLDS4_BODY(" sc1"); }
DI bf16x8 ldsr128(lds_cptr p) { return *(LAS const bf16x8*)p; }
DI s16x4 trd(lds_cptr p) { return __builtin_amdgcn_ds_read_tr16_b64_v4i16((LAS s16x4*)p); }
DI bf16x8 trd2(lds_cptr p, int second) {
    s16x4 lo = trd(p), hi = trd(p + second);
    return __builtin_shufflevector(lo, hi, 0, 1, 2, 3, 4, 5, 6, 7);
}
DI f32x16 mish16(const f32x16& h) {
    f32x16 e = h * LOG2E;
#pragma unroll
    for (int i = 0; i < 16; ++i) e[i] = __builtin_amdgcn_exp2f(e[i]);
    const f32x16 mh = {-0.5f, -0.5f, -0.5f, -0.5f, -0.5f, -0.5f, -0.5f, -0.5f, -0.5f, -0.5f, -0.5f, -0.5f, -0.5f, -0.5f, -0.5f, -0.5f};
    const f32x16 m1 = mh + mh;
    f32x16 d = __builtin_elementwise_fma(e, __builtin_elementwise_fma(e, mh, m1), m1);
#pragma unroll
    for (int i = 0; i < 16; ++i) d[i] = __builtin_amdgcn_rcpf(d[i]);
    return __builtin_elementwise_fma(h, d, h);
}
DI bool vm_idle() {
    unsigned r;
    asm volatile("s_getreg_b32 %0, hwreg(HW_REG_IB_STS, 0, 4)" : "=s"(r)::"memory");
    return r == 0;
}
DI void sig(lds_ptr sy, int off, int v) {
    asm volatile("s_mov_b64 exec, 1\n\tds_write_b32 %0, %1\n\ts_mov_b64 exec, -1" ::"v"((unsigned)(uintptr_t)(sy + off)), "v"(v) : "memory");
}
#define UNLIKELY(x) __builtin_expect(!!(x), 0)
DI void spin1(lds_ptr sy, int off, int need) {
    while (RFL(*(LAS volatile int*)(sy + off)) < need) __builtin_amdgcn_s_sleep(1);
    asm volatile("" ::: "memory");
}
DI int min4(lds_ptr sy, int off) {
    const i32x4 v = *(LAS volatile i32x4*)(sy + off);
    return RFL(min(min(v[0], v[1]), min(v[2], v[3])));
}
DI int min8(lds_ptr sy, int off) {
    const i32x4 v = *(LAS volatile i32x4*)(sy + off), u = *(LAS volatile i32x4*)(sy + off + 16);
    return RFL(min(min(min(v[0], v[1]), min(v[2], v[3])), min(min(u[0], u[1]), min(u[2], u[3]))));
}
DI void spin4(lds_ptr sy, int off, int need) {
    while (min4(sy, off) < need) __builtin_amdgcn_s_sleep(1);
    asm volatile("" ::: "memory");
}
DI void spin4f(lds_ptr sy, int off, int need) {
    while (min4(sy, off) < need) {}
    asm volatile("" ::: "memory");
}
DI void lds_add1(lds_ptr sy, int off) {
    asm volatile("s_mov_b64 exec, 1\n\tds_add_u32 %0, %1\n\ts_mov_b64 exec, -1" ::"v"((unsigned)(uintptr_t)(sy + off)), "v"(1) : "memory");
}
DI int lds_inc(lds_ptr sy, int off) {
    int r = 0;
    if ((threadIdx.x & 63) == 0) r = __hip_atomic_fetch_add((LAS int*)(sy + off), 1, __ATOMIC_RELAXED, __HIP_MEMORY_SCOPE_WORKGROUP);
    return RFL(r);
}
#define POLL_SLEEP 2
DI void flag_set(char* ws, unsigned off, u64 tag) {
    if ((threadIdx.x & 63) == 0) __hip_atomic_store((GAS u64*)(ws + off), tag, __ATOMIC_RELAXED, __HIP_MEMORY_SCOPE_AGENT);
}
template <int N>
DI void flag_wait(char* ws, unsigned laneoff, u64 tag) {
    GAS u64* p = (GAS u64*)(ws + laneoff);
    for (int spins = 0; spins < (1 << 20); ++spins) {
        bool ok = true;
#pragma unroll
        for (int i = 0; i < N; ++i) ok &= __hip_atomic_load(p + i * 64, __ATOMIC_RELAXED, __HIP_MEMORY_SCOPE_AGENT) == tag;
        if (__all(ok)) break;
        __builtin_amdgcn_s_sleep(POLL_SLEEP);
    }
    asm volatile("" ::: "memory");
}
#define WS_STORE(v, off) __builtin_amdgcn_raw_buffer_store_b128((v), wsr, (off), 0, 16)
#define WS_LOAD(voff, soff) __builtin_amdgcn_raw_buffer_load_b128(wsr, (voff), (soff), 16)

struct Geo {
    int lane, w, wm, h, l31, rot, q, tcol, sw;
};

template <int NB, bool SWAP, bool GATE, class Init, class Hook>
DI void proj_pass(lds_ptr lds, const Geo& g, int woff, f32x16 (&acc)[2], Init init, Hook hook) {
    const int xr = g.wm * 32 + g.l31;
    int xo[4];
#pragma unroll
    for (int s = 0; s < 4; ++s) xo[s] = L_RING + 16384 + xr * 128 + (((2 * s + g.h) ^ ((xr >> 1) & 7)) << 4);
    const int w0 = (L_RING + (8 * g.h + g.q) * 128 + g.sw + g.tcol) ^ woff, w1 = w0 ^ 64;
    constexpr int NF = NB + (GATE ? 1 : 0);
    bf16x8 xf[4], wf[4][NF];
    auto rd = [&](int s, int b, int t) {
        const int so = (t & 3) * 32768;
        if (b == 0) xf[s] = ldsr128(lds + xo[s] + so);
        if (b < NB) wf[s][b] = trd2(lds + ((b ? w1 : w0) + so) + s * 2048, 512);
        else wf[s][b] = ldsr128(lds + L_GATE + ((t + g.rot) & 7) * 128 + g.h * 16 + s * 32);
        SBAR();
    };
    auto mm = [&](int s, int b) {
        acc[b] = SWAP ? MFMA(xf[s], wf[s][b], acc[b]) : MFMA(wf[s][b], xf[s], acc[b]);
        SBAR();
    };
    LGKM_BAR(); SBAR();
    init();
#pragma unroll
    for (int s = 0; s < 4; ++s)
#pragma unroll
        for (int b = 0; b < NF; ++b) { if (s >= 2) mm(s - 2, b); rd(s, b, 0); }
#pragma unroll 1
    for (int t = 1; t < 8; ++t) {
        hook();
        LGKM_BAR(); SBAR();
#pragma unroll
        for (int s = 0; s < 4; ++s)
#pragma unroll
            for (int b = 0; b < NF; ++b) { mm((s + 2) & 3, b); rd(s, b, t); }
    }
#pragma unroll
    for (int s = 2; s < 4; ++s)
#pragma unroll
        for (int b = 0; b < NF; ++b) mm(s, b);
}
DI f32x16 bias16(lds_cptr p) {
    f32x16 v;
#pragma unroll
    for (int i = 0; i < 4; ++i) {
        const u32x2 bb = *(LAS const u32x2*)(p + 16 * i);
        v[4 * i] = bflo(bb[0]); v[4 * i + 1] = bfhi(bb[0]); v[4 * i + 2] = bflo(bb[1]); v[4 * i + 3] = bfhi(bb[1]);
    }
    return v;
}

#define LOOKA 8
#define PACE 2

__global__ __launch_bounds__(768, 3) void fused_block(const char* __restrict__ x, const char* __restrict__ wqkv, const char* __restrict__ wo,
                                                      const char* __restrict__ w1, const char* __restrict__ b1, const char* __restrict__ w2,
                                                      const char* __restrict__ gate, char* __restrict__ ws, char* __restrict__ out) {
    __shared__ __attribute__((aligned(1024))) char ldsmem[L_TOTAL];
    const int tid = threadIdx.x, lane = tid & 63, w = RFL(tid >> 6), wm = w & 3, h = lane >> 5, l31 = lane & 31;
    const int bid = blockIdx.x, xcd = bid & 7, idx = bid >> 3;
    const unsigned lds0 = (unsigned)(uintptr_t)ldsmem;
    const lds_ptr lds = (lds_ptr)ldsmem;
    const lds_ptr sy = lds + L_SYNC;
    const u64 dg_t0 = __builtin_amdgcn_s_memrealtime();
    if (tid == 0) __hip_atomic_store((GAS u64*)(ws + 24 * MB) + bid, dg_t0, __ATOMIC_RELAXED, __HIP_MEMORY_SCOPE_AGENT);
    u64 code_pc;
    unsigned code_len;
    asm volatile("s_getpc_b64 %0\n.Lcode_here:\n\ts_max_i32 %1, .Lcode_end_a-.Lcode_here, 0\n\ts_max_i32 %1, .Lcode_end_b-.Lcode_here, %1"
                 : "=s"(code_pc), "=&s"(code_len)::"scc");
    const int rb = (xcd & 3) * 4 + (idx & 3), c = (xcd >> 2) * 8 + (idx >> 2), j = c >> 1, odd = c & 1;
    const int rot = ((idx >> 2) + 2 * (idx & 3)) & 7;
    const int nh4 = ((j ^ c) & 1) == 0;
    const int hb0 = 7 * j + (odd ? (nh4 ? 3 : 4) : 0), hl0 = hb0 & ~1;
    const int head = xcd * 4 + (idx >> 3), qblk = idx & 7;
    const int mt = xcd * 4 + (idx >> 3), nt = idx & 7;
    u64 tag;
#define EPOCH_LOAD() asm volatile("s_load_dwordx2 %0, %1, 0x0" : "=&s"(tag) : "s"(ws + OFF_EP) : "memory")
#define EPOCH_WAIT() do { asm volatile("s_waitcnt lgkmcnt(0)" : "+s"(tag)::"memory"); tag += 1; } while (0)
    const __amdgpu_buffer_rsrc_t wsr = __builtin_amdgcn_make_buffer_rsrc(ws, 0, WS_USED, 0x00020000);
    const int wch = ((lane & 7) ^ (((lane >> 4) & 1) << 2)) * 16;
    const char* kg = ws + OFF_K + (size_t)head * 65536;
    const char* vg = ws + OFF_V + (size_t)head * 65536;

    if (__builtin_expect(w >= 8, 1)) {
        const int r8 = lane >> 3;
        const unsigned vx = (wm * 32 + r8) * 1024, cx0 = ((lane & 7) ^ (r8 >> 1)) << 4, cx1 = cx0 ^ 64;
        const unsigned vx0 = vx + cx0, vx1 = vx + 7168 + cx1, vx2 = vx + 2 * 7168 + cx0, vx3 = vx + 3 * 7168 + cx1;
        const unsigned vq = ((wm & 1) * 32 + r8) * 3072 + wch + (wm >> 1) * 1024, vh = ((wm & 1) * 32 + r8) * 3584 + wch + (wm >> 1) * 128;
        const char* xb = x + (size_t)rb * 128 * 1024;
        const char* qb = wqkv + j * 128 + odd * 1024;
        const char* hb = w1 + hl0 * 64;
        const unsigned rd0 = lds0 + L_RING + wm * 4096;
        auto unit = [&](int u) {
            const int ks = ((u >> 1) + rot) & 7;
            const unsigned d = rd0 + (u % NSLOT) * 16384;
            if (u & 1) glds4(xb + ks * 128, vx0, vx1, vx2, vx3, d);
            else if (u < 16) glds4(qb + ks * (64 * 3072), vq, vq + (8 * 3072 - 1024), vq + 2 * (8 * 3072 - 1024), vq + 3 * (8 * 3072 - 1024), d);
            else glds4(hb + ks * (64 * 3584), vh, vh + (8 * 3584 - 1024), vh + 2 * (8 * 3584 - 1024), vh + 3 * (8 * 3584 - 1024), d);
        };
        { const unsigned o1 = (lane & 11) == 0 ? 0x3f803f80u : 0u; *(LAS u32x4*)(lds + L_ONES + (tid - 512) * 16) = u32x4{o1, o1, o1, o1}; }
        if (w == 8) *(LAS int*)(sy + lane * 4) = 0;
        __builtin_amdgcn_s_setprio(1);
        unit(0); unit(1);
        EPOCH_LOAD();
        unit(2);
        EPOCH_WAIT();
        STAMP();
        VM_BAR(4);
        STAMP();
        unit(3); unit(4); unit(5); unit(6); unit(7);
        unsigned pf;
        {
            const int pt = bid < 224 ? bid : 223;
            const char* s2 = w2 + (size_t)(pt >> 3) * 65536 + (pt & 7) * 128;
            const unsigned v2 = (wm * 16 + r8) * 1024 + wch;
            glds16(s2, v2, lds0 + L_W2 + wm * 2048); glds16(s2 + 8192, v2, lds0 + L_W2 + wm * 2048 + 1024);
            if (wm & 1) glds1(b1 + hl0 * 64, lane * 4, lds0 + L_BIAS); else glds16(gate, lane * 16, lds0 + L_GATE);
            asm volatile("global_load_dword %0, %1, %2" : "=v"(pf) : "v"(((idx & 3) * 128 + wm * 32 + l31) * 3584 + h * 128), "s"(hb) : "memory");
        }
#pragma unroll
        for (int t = 1; t < 16; ++t) {
            if (t < 4) VM_BAR(20); else if (t < 14) VM_BAR(16); else if (t == 14) VM_BAR(8); else VM_BAR(0);
            if (2 * t + 6 < 32) { unit(2 * t + 6); unit(2 * t + 7); }
            if (t == 4 || t == 8 || t == 12) STAMP();
        }
        PIN(pf);
        STAMP();
        if (w == 8) { flag_wait<1>(ws, OFF_FQ + (head >> 2) * 256 + l31 * 8, tag); sig(sy, S_QOK, 1); }
        const char* kvg = (wm & 1) ? vg : kg;
        const unsigned kvd = lds0 + L_KV + (wm & 1) * 32768 + (wm >> 1) * 4096;
        asm volatile("s_barrier" ::: "memory");
        spin1(sy, S_QOK, 1);
        if (wm < 2) glds4c(ws + OFF_Q + (size_t)(head * SEQ + qblk * 256) * 32 + wm * 4096, lane * 16, lds0 + L_X + wm * 4096);
#pragma unroll
        for (int i = 0; i < 8; ++i) {
            const int hf = i < 2 ? i : (i < 5 ? 0 : 1), gi = i < 2 ? 0 : (i < 5 ? i - 1 : i - 4);
            glds4c(kvg + hf * 32768 + (((wm >> 1) + 2 * gi + qblk) & 7) * 4096, lane * 16, kvd + hf * 65536 + gi * 8192);
            if (i == 2) { VMW(8); sig(sy, S_KVM + wm * 4, 1); }
            if (i == 3) { VMW(8); sig(sy, S_KVM + wm * 4, 2); }
            if (i == 6) { VMW(8); sig(sy, S_KVM + wm * 4, 3); }
        }
        VMW(0); sig(sy, S_KVM + wm * 4, 4);
        STAMP();
        __builtin_amdgcn_s_setprio(3);
        if (w == 8) {
            flag_wait<1>(ws, h ? OFF_FH + ((mt >> 1) * 16 + (lane & 15)) * 8 : OFF_FW + (nt * 32 + (l31 < 28 ? l31 : 0)) * 8, tag);
            sig(sy, S_HOK, 1);
        } else spin1(sy, S_HOK, 1);
        STAMP();
        constexpr int PD = 4;
        const int hs = OFF_H + ((mt * 2 + (wm & 1)) * 112 + wm * 28) * 1024, hs1 = OFF_H + ((mt * 2 + ((wm & 1) ^ 1)) * 112 + wm * 28) * 1024;
        const int wso = OFF_W2F + ((nt * 2 + (wm >> 1)) * 112 + wm * 28) * 1024, wso1 = OFF_W2F + ((nt * 2 + ((wm >> 1) ^ 1)) * 112 + wm * 28) * 1024;
        u32x4 hf[PD][2], wf[PD][2];
        auto ld = [&](int i, int k) {
            hf[i][0] = WS_LOAD(lane * 16, hs + k * 1024); wf[i][0] = WS_LOAD(lane * 16, wso + k * 1024);
            hf[i][1] = WS_LOAD(lane * 16, hs1 + k * 1024); wf[i][1] = WS_LOAD(lane * 16, wso1 + k * 1024);
            SBAR();
        };
#pragma unroll
        for (int i = 0; i < PD; ++i) ld(i, i);
        f32x16 acc[2][2] = {};
        bool wopend = true;
        auto load_wo = [&] {
            const char* wg = wo + nt * 128 + (size_t)wm * 131072;
            const unsigned vow = r8 * 1024 + wch;
#pragma unroll
            for (int i = 0; i < 4; ++i) glds4(wg + i * 32768, vow, vow + 7168, vow + 2 * 7168, vow + 3 * 7168, lds0 + L_KV + wm * 32768 + i * 4096);
            wopend = false;
        };
#pragma unroll 1
        for (int k0 = 0; k0 < 28; k0 += PD) {
#pragma unroll
            for (int i = 0; i < PD; ++i) {
                const u32x2 st2 = *(LAS volatile u32x2*)(sy + S_DONE);
                const bool slow = RFL(st2[0]) < 8;
                if (UNLIKELY(wopend && RFL(st2[1]) == 8)) load_wo();
#pragma unroll
                for (int n = 0; n < 2; ++n)
#pragma unroll
                    for (int m = 0; m < 2; ++m) {
                        acc[n][m] = MFMA(BC8(wf[i][n]), BC8(hf[i][m]), acc[n][m]);
                        SBAR(); if (slow) __builtin_amdgcn_s_sleep(PACE); SBAR();
                    }
                const int kn = k0 + PD + i;
                ld(i, kn < 28 ? kn : 27);
            }
        }
        STAMP();
        if (wopend) { spin1(sy, S_P16, 8); load_wo(); }
        VMW(0);
        spin1(sy, S_P24, 8);
        f32x16 keep = acc[0][0];
#pragma unroll
        for (int r = 1; r < 4; ++r) {
            const f32x16& gv = acc[r >> 1][r & 1];
            if (r == 3) spin1(sy, S_EX + (wm ^ 1) * 4, 2);
            LAS f32x4* dst = (LAS f32x4*)(lds + L_KV + wm * 32768 + 16384 + (r & 1) * 4096) + lane;
#pragma unroll
            for (int i = 0; i < 4; ++i) dst[i * 64] = f32x4{gv[4 * i], gv[4 * i + 1], gv[4 * i + 2], gv[4 * i + 3]};
            LGW(); sig(sy, S_EX + wm * 4, r);
            spin1(sy, S_EX + (wm ^ r) * 4, r);
            LAS const f32x4* src = (LAS const f32x4*)(lds + L_KV + (wm ^ r) * 32768 + 16384 + (r & 1) * 4096) + lane;
#pragma unroll
            for (int i = 0; i < 4; ++i) {
                const f32x4 v = src[i * 64];
#pragma unroll
                for (int e = 0; e < 4; ++e) keep[4 * i + e] += v[e];
            }
        }
        { LAS u32x4* pb = (LAS u32x4*)(lds + L_W2 + wm * 2048) + lane; pb[0] = pk8(keep, 0); pb[64] = pk8(keep, 1); }
        LGW(); sig(sy, S_FB + wm * 4, 1);
        if (bid == 0 && w == 8) {
            flag_wait<4>(ws, OFF_FH + lane * 8, tag);
            flag_set(ws, OFF_EP, tag);
        }
        STAMP(); STAMP_RT(); STAMP_OUT(0, ws);
        asm volatile(".Lcode_end_a:");
        return;
    }

    Geo g;
    g.lane = lane; g.w = w; g.wm = wm; g.h = h; g.l31 = l31; g.rot = rot;
    g.q = (lane & 15) >> 2; g.tcol = ((lane >> 4) & 1) * 32 + (lane & 3) * 8; g.sw = (g.q >> 1) << 6;
    const int tok0 = rb * 128 + wm * 32, half = w >> 2;
    unsigned cpf = 0;
    if (w == 0) {
        const unsigned o = (lane * 32 + idx) * 128;
        if (o < code_len) asm volatile("global_load_dword %0, %1, %2" : "=v"(cpf) : "v"(o), "s"(code_pc) : "memory");
    }
    EPOCH_LOAD();
    EPOCH_WAIT();
    __builtin_amdgcn_s_setprio(2);
    {
        f32x16 acc[2] = {};
        auto nohook = [] {};
        const unsigned o = (4 * j * SEQ + tok0 + l31) * 32 + h * 16;
        if (half) {
            proj_pass<1, false, false>(lds, g, odd ? 64 : 8192, acc, nohook, nohook);
            STAMP();
#pragma unroll
            for (int s = 0; s < 2; ++s) WS_STORE(pk8(acc[0], s), OFF_K + o + (2 * odd + s) * SEQ * 32);
        } else if (!odd) {
            proj_pass<2, false, false>(lds, g, 0, acc, nohook, nohook);
            STAMP();
#pragma unroll
            for (int i = 0; i < 4; ++i) WS_STORE(pk8(acc[i >> 1] * QSCALE, i & 1), OFF_Q + o + i * SEQ * 32);
        } else {
            proj_pass<2, true, false>(lds, g, 8192, acc, nohook, nohook);
            STAMP();
            const unsigned ov = OFF_V + ((4 * j + (l31 >> 4)) * 128 + (tok0 >> 4)) * 512 + h * 256 + (lane & 15) * 16;
#pragma unroll
            for (int i = 0; i < 4; ++i) WS_STORE(pk8(acc[i >> 1], i & 1), ov + (i >> 1) * 2 * 65536 + (i & 1) * 512);
        }
    }
    WS_STORE((u32x4{POISON, POISON, POISON, POISON}), OFF_O + (((rb * 4 + wm) * 32 + 4 * j + 2 * odd + half) * 64 + lane) * 16);
    STAMP();
    {
        bool qpend = true;
        auto pubq_now = [&] {
            VMW(0); PIN(cpf);
            if (lds_inc(sy, S_CNTQ) == 7) flag_set(ws, OFF_FQ + ((j * 16 + rb) * 2 + odd) * 8, tag);
            qpend = false;
        };
        auto pubq = [&] { if (UNLIKELY(qpend && vm_idle())) pubq_now(); };
        const int pB = (nh4 || (hb0 & 1)) ? 1 : 0;
        const int blk = half ? 2 * pB : (nh4 ? 0 : (pB ? 1 : 2));
        const int nb = (half || nh4) ? 2 : 1;
        f32x16 acc[2];
        const int woff = (blk >> 1) * 8192 + (blk & 1) * 64;
        auto init2 = [&] { acc[0] = bias16(lds + L_BIAS + blk * 64 + h * 8); acc[1] = bias16(lds + L_BIAS + blk * 64 + 64 + h * 8); };
        auto init1 = [&] { acc[0] = bias16(lds + L_BIAS + blk * 64 + h * 8); acc[1] = f32x16{}; };
        if (nb == 2) proj_pass<2, false, false>(lds, g, woff, acc, init2, pubq);
        else if (c == 1) proj_pass<1, false, true>(lds, g, woff, acc, init1, pubq);
        else proj_pass<1, false, false>(lds, g, woff, acc, init1, pubq);
        LGKM_BAR();
        if (qpend) pubq_now();
        STAMP();
        const unsigned oh = OFF_H + (((tok0 >> 5) * 112 + (hl0 + blk) * 2) * 64 + lane) * 16;
        { const f32x16 v = mish16(acc[0]); WS_STORE(pk8(v, 0), oh); WS_STORE(pk8(v, 1), oh + 1024); }
        if (nb == 2) { const f32x16 v = mish16(acc[1]); WS_STORE(pk8(v, 0), oh + 2048); WS_STORE(pk8(v, 1), oh + 3072); }
        else if (c == 1) {
            const float e = __builtin_amdgcn_exp2f(acc[1][0] * -LOG2E);
            if (h == 0) __hip_atomic_store((GAS float*)(ws + OFF_G) + tok0 + l31, __builtin_amdgcn_rcpf(1.0f + e), __ATOMIC_RELAXED, __HIP_MEMORY_SCOPE_AGENT);
        }
        if (bid < 224) {
            const bf16x8 f = trd2(lds + L_W2 + (16 * wm + 4 * h + g.q) * 128 + g.tcol + (g.sw ^ (half * 64)), 1024);
            WS_STORE(__builtin_bit_cast(u32x4, f), OFF_W2F + ((((bid & 7) * 2 + half) * 112 + (bid >> 3) * 4 + wm) * 64 + lane) * 16);
        }
    }
    STAMP();
    bool hpend = true;
    auto pubh_now = [&] {
        VMW(0);
        if (lds_inc(sy, S_CNTH) == 7) {
            flag_set(ws, OFF_FH + (rb * 16 + c) * 8, tag);
            if (bid < 224) flag_set(ws, OFF_FW + ((bid & 7) * 32 + (bid >> 3)) * 8, tag);
        }
        hpend = false;
    };
    auto pubh = [&] { if (UNLIKELY(hpend && vm_idle())) pubh_now(); };
    while (min4(sy, S_KVM) < 1 + half) { pubh(); __builtin_amdgcn_s_sleep(1); }
    asm volatile("" ::: "memory");
    STAMP();
    const bf16x8 qA = ldsr128(lds + L_X + wm * 2048 + l31 * 32 + h * 16), qB = ldsr128(lds + L_X + wm * 2048 + 1024 + l31 * 32 + h * 16);
    const lds_cptr kp = lds + L_KV + half * 65536 + l31 * 32 + h * 16;
    const bool isone = (lane & 16) != 0;
    lds_cptr vp = lds + (isone ? L_ONES : L_KV + half * 65536 + 32768) + h * 256 + (lane & 15) * 16;
    const int vstep = isone ? 0 : 4096;
    f32x16 OA = {}, OB = {}, SA, SB;
    const f32x16 Z = {};
    bf16x8 kf = ldsr128(kp), v0 = ldsr128(vp), v1 = ldsr128(vp + 512);
    u32x4 p0, p1 = {0, 0, 0, 0};
    SA = MFMA(kf, qA, Z);
#define EX(i) S_[i] = __builtin_amdgcn_exp2f(S_[i])
#define UNIT(S, SN, QN, O, OP, LD0, LD1, LD2)                                                       \
    {                                                                                               \
        f32x16& S_ = S;                                                                             \
        SN = MFMA(kf, QN, Z); PIN(SN); SBAR();                                                      \
        LD0; EX(0); EX(1); PIN(S_); SBAR();                                                         \
        OP = MFMA(v1, BC8(p1), OP); PIN(OP); SBAR();                                                \
        LD1; EX(2); EX(3); EX(4); EX(5); EX(6); EX(7); PIN(S_); SBAR();                             \
        p0 = pk8(S_, 0); PIN(p0); SBAR();                                                           \
        EX(8); EX(9); PIN(S_); SBAR();                                                              \
        O = MFMA(v0, BC8(p0), O); PIN(O); SBAR();                                                   \
        LD2; EX(10); EX(11); EX(12); EX(13); EX(14); EX(15); PIN(S_); SBAR();                       \
        p1 = pk8(S_, 1); PIN(p1); SBAR();                                                           \
    }
#define TILE(t, u)                                                                                  \
    UNIT(SA, SB, qB, OA, OB, kf = ldsr128(kp + (((t) + 1) & 31) * 1024), v1 = ldsr128(vp + (u) * 1024 + 512), (void)0) \
    UNIT(SB, SA, qA, OB, OA, (void)0, (void)0, v0 = ldsr128((u) == 3 ? vp + vstep : vp + ((u) + 1) * 1024))
    __builtin_amdgcn_s_setprio(2);
#pragma unroll 1
    for (int t = 0; t < 32; t += 4) {
        if (t == 4) { lds_add1(sy, S_QRD); spin4(sy, S_KVM, 3 + half); }
        if (t == 12) __builtin_amdgcn_s_setprio(1);
        if (t == 16) lds_add1(sy, S_P16);
        if (t == 24) { __builtin_amdgcn_s_setprio(0); lds_add1(sy, S_P24); spin1(sy, S_QRD, 8); }
        TILE(t, 0) pubh(); TILE(t + 1, 1) pubh(); TILE(t + 2, 2) pubh(); TILE(t + 3, 3) pubh();
        vp += vstep;
    }
    if (hpend) pubh_now();
    OB = MFMA(v1, BC8(p1), OB);
    PIN(kf); PIN(v0);
    LGW();
    lds_add1(sy, S_DONE);
    STAMP();
    {
        const f32x16 give = half ? OA : OB;
        LAS f32x4* dst = (LAS f32x4*)(lds + L_X + w * 2048) + lane;
        dst[0] = f32x4{give[0], give[1], give[2], give[3]};
        dst[64] = f32x4{give[4], give[5], give[6], give[7]};
        ((LAS float*)(lds + L_GATE + w * 128))[l31] = give[8];
        LGW(); sig(sy, S_FX + w * 4, 1);
    }
    f32x16 O = half ? OB : OA;
    spin1(sy, S_FX + (w ^ 4) * 4, 1);
    {
        LAS const f32x4* src = (LAS const f32x4*)(lds + L_X + (w ^ 4) * 2048) + lane;
        const f32x4 a = src[0], b = src[64];
#pragma unroll
        for (int r = 0; r < 4; ++r) { O[r] += a[r]; O[4 + r] += b[r]; }
        O[8] += ((LAS const float*)(lds + L_GATE + (w ^ 4) * 128))[l31];
    }
    bool bad = !(O[8] > 0x1p-90f && O[8] < 0x1p100f);
#pragma unroll
    for (int r = 0; r < 8; ++r) bad |= !(__builtin_fabsf(O[r]) < 0x1p120f);
    if (__any(bad)) {
        const bf16x8 qf = half ? qB : qA;
        const unsigned o1 = (lane & 11) == 0 ? 0x3f803f80u : 0u;
        float m = -INFINITY;
        O = Z;
#pragma unroll 1
        for (int t = 0; t < 64; ++t) {
            f32x16 s = MFMA(BC8(WS_LOAD(l31 * 32 + h * 16, OFF_K + head * 65536 + t * 1024)), qf, Z);
            u32x4 va = WS_LOAD(h * 256 + (lane & 15) * 16, OFF_V + head * 65536 + t * 1024);
            u32x4 vb = WS_LOAD(h * 256 + (lane & 15) * 16, OFF_V + head * 65536 + t * 1024 + 512);
            if (isone) { va = u32x4{o1, o1, o1, o1}; vb = va; }
            float tm = s[0];
#pragma unroll
            for (int r = 1; r < 16; ++r) tm = __builtin_fmaxf(tm, s[r]);
            tm = __builtin_fmaxf(tm, __shfl_xor(tm, 32));
            const float mn = __builtin_fmaxf(m, tm);
            const float al = __builtin_amdgcn_exp2f(m - mn);
            m = mn;
#pragma unroll
            for (int r = 0; r < 16; ++r) { O[r] *= al; s[r] = __builtin_amdgcn_exp2f(s[r] - mn); }
            O = MFMA(BC8(va), BC8(pk8(s, 0)), O);
            O = MFMA(BC8(vb), BC8(pk8(s, 1)), O);
        }
    }
    {
        u32x4 ov = pk8(O * __builtin_amdgcn_rcpf(O[8]), 0);
#pragma unroll
        for (int i = 0; i < 4; ++i) ov[i] = ov[i] == POISON ? 0x7fff7fffu : ov[i];
        WS_STORE(ov, OFF_O + (((qblk * 8 + wm * 2 + half) * 32 + head) * 64 + lane) * 16);
    }
    sig(sy, S_FM + w * 4, 1);
    STAMP();
    const int wn = (w >> 1) & 1, wmm = w & 1;
    const int row = mt * 64 + wmm * 32 + l31;
    float gv = 0.f;
    spin4(sy, S_FB, 1);
    spin1(sy, S_DONE, 8);
    if (w < 4) gv = __hip_atomic_load((GAS float*)(ws + OFF_G) + row, __ATOMIC_RELAXED, __HIP_MEMORY_SCOPE_AGENT);
    const char* og = ws + OFF_O + (size_t)mt * 65536;
    {
        const int kq = ((w >> 1) + nt) & 3;
        const char* src = og + wmm * 32768 + kq * 8192;
        const unsigned dst = lds0 + L_KV + 16384 + kq * 32768 + wmm * 8192;
        glds4c(src, lane * 16, dst); glds4c(src + 4096, lane * 16, dst + 4096);
    }
    STAMP();
    VMW(0); sig(sy, (w < 4 ? S_OF : S_OF2 - 16) + w * 4, 1);
    spin4(sy, S_OF, 1); spin4(sy, S_OF2, 1);
    STAMP();
    const lds_cptr wa = lds + L_KV + half * 65536 + (4 * h + g.q) * 128 + (g.sw ^ (wn * 64)) + g.tcol;
    const lds_cptr oa = lds + L_KV + 16384 + half * 65536 + wmm * 8192 + lane * 16;
    f32x16 acc = {};
    int budget = 1 << 18;
#pragma unroll
    for (int q4 = 0; q4 < 4; ++q4) {
        u32x4 of[4];
        bf16x8 wv[4];
#pragma unroll
        for (int i = 0; i < 4; ++i) {
            of[i] = *(LAS const u32x4*)(oa + (q4 >> 1) * 32768 + ((q4 & 1) * 4 + i) * 1024);
            wv[i] = trd2(wa + (q4 >> 1) * 32768 + ((q4 & 1) * 4 + i) * 2048, 1024);
        }
        for (;;) {
            unsigned mx = 0;
#pragma unroll
            for (int i = 0; i < 4; ++i) mx = max(max(mx, max(of[i][0], of[i][1])), max(of[i][2], of[i][3]));
            if (!UNLIKELY(__any(mx == POISON)) || --budget < 0) break;
            __builtin_amdgcn_s_sleep(POLL_SLEEP);
            glds4c(og + wmm * 32768 + (half * 16 + q4 * 4) * 1024, lane * 16, lds0 + L_KV + 16384 + (half * 2 + (q4 >> 1)) * 32768 + wmm * 8192 + (q4 & 1) * 4096);
            VMW(0);
#pragma unroll
            for (int i = 0; i < 4; ++i) of[i] = *(LAS const u32x4*)(oa + (q4 >> 1) * 32768 + ((q4 & 1) * 4 + i) * 1024);
        }
#pragma unroll
        for (int i = 0; i < 4; ++i) acc = MFMA(wv[i], BC8(of[i]), acc);
    }
    STAMP();
    LAS f32x4* ex = (LAS f32x4*)(lds + L_X + wm * 4096) + lane;
    if (w >= 4) {
        spin4(sy, S_FM, 1); spin4(sy, S_FM + 16, 1);
#pragma unroll
        for (int i = 0; i < 4; ++i) ex[i * 64] = f32x4{acc[4 * i], acc[4 * i + 1], acc[4 * i + 2], acc[4 * i + 3]};
        LGW(); sig(sy, S_FY + wm * 4, 1);
    } else {
        spin1(sy, S_FY + wm * 4, 1);
#pragma unroll
        for (int i = 0; i < 4; ++i) {
            const f32x4 r = ex[i * 64];
#pragma unroll
            for (int e = 0; e < 4; ++e) acc[4 * i + e] += r[e];
        }
        f32x16 bq;
        {
            LAS const u32x4* pb = (LAS const u32x4*)(lds + L_W2 + wm * 2048) + lane;
            const u32x4 b0 = pb[0], b1v = pb[64];
#pragma unroll
            for (int i = 0; i < 4; ++i) { bq[2 * i] = bflo(b0[i]); bq[2 * i + 1] = bfhi(b0[i]); bq[8 + 2 * i] = bflo(b1v[i]); bq[8 + 2 * i + 1] = bfhi(b1v[i]); }
        }
        const f32x16 res = acc * gv + bq * (1.0f - gv);
        const u32x4 lo = pk8(res, 0), hi = pk8(res, 1);
#pragma unroll
        for (int k = 0; k < 2; ++k) {
            const u32x4 o = k ? hi : lo;
            auto r0 = __builtin_amdgcn_permlane32_swap(o[0], o[2], false, false);
            auto r1 = __builtin_amdgcn_permlane32_swap(o[1], o[3], false, false);
            *(GAS u32x4*)(out + (size_t)row * 1024 + (nt * 64 + wn * 32 + 16 * k + 8 * h) * 2) = u32x4{r0[0], r1[0], r0[1], r1[1]};
        }
    }
    STAMP(); STAMP_RT(); STAMP_OUT(0, ws);
    if (bid == DGB && w == 4) {
        u64 mn = ~0ull, mx = 0;
        for (int i = 0; i < 4; ++i) { const u64 v = __hip_atomic_load((GAS u64*)(ws + 24 * MB) + i * 64 + lane, __ATOMIC_RELAXED, __HIP_MEMORY_SCOPE_AGENT); mn = v < mn ? v : mn; mx = v > mx ? v : mx; }
        for (int o = 1; o < 64; o <<= 1) { const u64 a = __shfl_xor(mn, o), b = __shfl_xor(mx, o); mn = a < mn ? a : mn; mx = b > mx ? b : mx; }
        const u64 now = __builtin_amdgcn_s_memrealtime();
        u64 x = mx - mn; if (x > 10000) x = 10000;
        const u64 e_ = now + 8 * x; while (__builtin_amdgcn_s_memrealtime() < e_) __builtin_amdgcn_s_sleep(4); }
    asm volatile(".Lcode_end_b:");
}

extern "C" void kernel_launch(void* const* d_in, const int* in_sizes, int n_in, void* d_out, int out_size, void* d_ws, size_t ws_size,
                              hipStream_t stream) {
    fused_block<<<256, 768, 0, stream>>>((const char*)d_in[0], (const char*)d_in[1], (const char*)d_in[2], (const char*)d_in[4],
                                         (const char*)d_in[5], (const char*)d_in[6], (const char*)d_in[7], (char*)d_ws, (char*)d_out);
}
```

```cpp
#include <hip/hip_runtime.h>
#include <hip/hip_bf16.h>
#include <stdint.h>

typedef short bf16x8 __attribute__((ext_vector_type(8)));
typedef short s16x4 __attribute__((ext_vector_type(4)));
typedef float f32x16 __attribute__((ext_vector_type(16)));
typedef float f32x2 __attribute__((ext_vector_type(2)));
typedef float f32x4 __attribute__((ext_vector_type(4)));
typedef __bf16 bf16x2_t __attribute__((ext_vector_type(2)));
typedef unsigned u32x4 __attribute__((ext_vector_type(4)));
typedef unsigned u32x2 __attribute__((ext_vector_type(2)));
typedef int i32x4 __attribute__((ext_vector_type(4)));
typedef unsigned long long u64;
#define LAS __attribute__((address_space(3)))
#define GAS __attribute__((address_space(1)))
typedef LAS const char* lds_cptr;
typedef LAS char* lds_ptr;
#define DI __device__ __forceinline__
#define MFMA(a, b, c) __builtin_amdgcn_mfma_f32_32x32x16_bf16((a), (b), (c), 0, 0, 0)
#define SBAR() __builtin_amdgcn_sched_barrier(0)
#define PIN(x) asm volatile("" : "+v"(x))
#define VMW(N) asm volatile("s_waitcnt vmcnt(%0)" ::"n"(N) : "memory")
#define LGKM_BAR() asm volatile("s_waitcnt lgkmcnt(0)\n\ts_barrier" ::: "memory")
#define VM_BAR(N) asm volatile("s_waitcnt vmcnt(%0)\n\ts_barrier" ::"n"(N) : "memory")
#define LGW() asm volatile("s_waitcnt lgkmcnt(0)" ::: "memory")
#define BC8(x) __builtin_bit_cast(bf16x8, (x))
#define RFL(x) __builtin_amdgcn_readfirstlane(x)

constexpr int SEQ = 2048;
constexpr unsigned MB = 1024 * 1024;
constexpr unsigned OFF_Q = 0, OFF_K = 2 * MB, OFF_V = 4 * MB, OFF_H = 6 * MB, OFF_W2F = 14 * MB, OFF_O = 16 * MB, OFF_G = 18 * MB,
                   OFF_FQ = 18 * MB + 16384, OFF_FH = OFF_FQ + 2048, OFF_FO = OFF_FH + 2048, OFF_FW = OFF_FO + 2048, OFF_EP = OFF_FW + 2048,
                   WS_USED = 19 * MB;
constexpr unsigned POISON = 0xffffffffu;
constexpr float LOG2E = 1.4426950408889634f;
constexpr float QSCALE = 0.25f * LOG2E;

constexpr int L_KV = 0;
constexpr int L_RING = 0, NSLOT = 8;
constexpr int L_X = 131072;
constexpr int L_W2 = 147456, L_GATE = 155648, L_ONES = 156672, L_SYNC = 160768, L_TOTAL = L_SYNC + 512;
constexpr int L_BIAS = L_SYNC + 256;
constexpr int S_EX = 0, S_PROG = 16  , S_KVM = 48, S_CNTQ = 84, S_CNTH = 88,
              S_HOK = 104, S_FX = 112  , S_FB = 144, S_OF = 160  , S_FY = 192, S_FM = 208  ;

#define TRACE(kid, i)
#define STAMP_DECL
#define STAMP()
#define STAMP1()
#define STAMP_RT()
#define STAMP_OUT(kid, wsp)

DI unsigned pk2(float a, float b) {
    f32x2 v = {a, b};
    return __builtin_bit_cast(unsigned, __builtin_convertvector(v, bf16x2_t));
}
DI u32x4 pk8(const f32x16& x, int s) {
    u32x4 r = {pk2(x[8 * s], x[8 * s + 1]), pk2(x[8 * s + 2], x[8 * s + 3]), pk2(x[8 * s + 4], x[8 * s + 5]), pk2(x[8 * s + 6], x[8 * s + 7])};
    return r;
}
DI float bflo(unsigned u) { return __uint_as_float(u << 16); }
DI float bfhi(unsigned u) { return __uint_as_float(u & 0xffff0000u); }
DI void glds16(const void* sbase, unsigned voff, unsigned lds_dst) {
    unsigned sv;
    asm volatile("s_mov_b32 %0, m0\n\ts_mov_b32 m0, %3\n\ts_nop 0\n\tglobal_load_lds_dwordx4 %1, %2\n\ts_mov_b32 m0, %0"
                 : "=&s"(sv) : "v"(voff), "s"(sbase), "s"(lds_dst) : "memory");
}
DI void glds1(const void* sbase, unsigned voff, unsigned lds_dst) {
    unsigned sv;
    asm volatile("s_mov_b32 %0, m0\n\ts_mov_b32 m0, %3\n\ts_nop 0\n\tglobal_load_lds_dword %1, %2\n\ts_mov_b32 m0, %0"
                 : "=&s"(sv) : "v"(voff), "s"(sbase), "s"(lds_dst) : "memory");
}
#define GLDS4_BODY(POL)                                                                                                    \
    unsigned sv;                                                                                                           \
    asm volatile("s_mov_b32 %0, m0\n\ts_mov_b32 m0, %6\n\ts_nop 0\n\tglobal_load_lds_dwordx4 %1, %5" POL                   \
                 "\n\tglobal_load_lds_dwordx4 %2, %5 offset:1024" POL "\n\tglobal_load_lds_dwordx4 %3, %5 offset:2048" POL \
                 "\n\tglobal_load_lds_dwordx4 %4, %5 offset:3072" POL "\n\ts_mov_b32 m0, %0"                               \
                 : "=&s"(sv) : "v"(v0), "v"(v1), "v"(v2), "v"(v3), "s"(sbase), "s"(lds_dst) : "memory")
DI void glds4(const void* sbase, unsigned v0, unsigned v1, unsigned v2, unsigned v3, unsigned lds_dst) { GLDS4_BODY(""); }
DI void glds4c(const void* sbase, unsigned v0, unsigned lds_dst) { const unsigned v1 = v0, v2 = v0, v3 = v0; GLDS4_BODY(" sc1"); }
DI bf16x8 ldsr128(lds_cptr p) { return *(LAS const bf16x8*)p; }
DI s16x4 trd(lds_cptr p) { return __builtin_amdgcn_ds_read_tr16_b64_v4i16((LAS s16x4*)p); }
DI bf16x8 trd2(lds_cptr p, int second) {
    s16x4 lo = trd(p), hi = trd(p + second);
    return __builtin_shufflevector(lo, hi, 0, 1, 2, 3, 4, 5, 6, 7);
}
DI f32x16 mish16(const f32x16& h) {
    f32x16 e = h * LOG2E;
#pragma unroll
    for (int i = 0; i < 16; ++i) e[i] = __builtin_amdgcn_exp2f(e[i]);
    const f32x16 mh = {-0.5f, -0.5f, -0.5f, -0.5f, -0.5f, -0.5f, -0.5f, -0.5f, -0.5f, -0.5f, -0.5f, -0.5f, -0.5f, -0.5f, -0.5f, -0.5f};
    const f32x16 m1 = mh + mh;
    f32x16 d = __builtin_elementwise_fma(e, __builtin_elementwise_fma(e, mh, m1), m1);
#pragma unroll
    for (int i = 0; i < 16; ++i) d[i] = __builtin_amdgcn_rcpf(d[i]);
    return __builtin_elementwise_fma(h, d, h);
}
DI unsigned vm_cnt4() {
    unsigned r;
    asm volatile("s_getreg_b32 %0, hwreg(HW_REG_IB_STS, 0, 4)" : "=s"(r)::"memory");
    return r;
}
DI void sig(lds_ptr sy, int off, int v) {
    asm volatile("s_mov_b64 exec, 1\n\tds_write_b32 %0, %1\n\ts_mov_b64 exec, -1" ::"v"((unsigned)(uintptr_t)(sy + off)), "v"(v) : "memory");
}
#define UNLIKELY(x) __builtin_expect(!!(x), 0)
DI void spin1(lds_ptr sy, int off, int need) {
    while (RFL(*(LAS volatile int*)(sy + off)) < need) __builtin_amdgcn_s_sleep(1);
    asm volatile("" ::: "memory");
}
DI int min4(lds_ptr sy, int off) {
    const i32x4 v = *(LAS volatile i32x4*)(sy + off);
    return RFL(min(min(v[0], v[1]), min(v[2], v[3])));
}
DI int min8(lds_ptr sy, int off) {
    const i32x4 v = *(LAS volatile i32x4*)(sy + off), u = *(LAS volatile i32x4*)(sy + off + 16);
    return RFL(min(min(min(v[0], v[1]), min(v[2], v[3])), min(min(u[0], u[1]), min(u[2], u[3]))));
}
DI void spin4(lds_ptr sy, int off, int need) {
    while (min4(sy, off) < need) __builtin_amdgcn_s_sleep(1);
    asm volatile("" ::: "memory");
}
DI void spin8(lds_ptr sy, int off, int need) {
    while (min8(sy, off) < need) __builtin_amdgcn_s_sleep(1);
    asm volatile("" ::: "memory");
}
DI void spin4f(lds_ptr sy, int off, int need) {
    while (min4(sy, off) < need) {}
    asm volatile("" ::: "memory");
}
DI void lds_add1(lds_ptr sy, int off) {
    asm volatile("s_mov_b64 exec, 1\n\tds_add_u32 %0, %1\n\ts_mov_b64 exec, -1" ::"v"((unsigned)(uintptr_t)(sy + off)), "v"(1) : "memory");
}
DI int lds_inc(lds_ptr sy, int off) {
    int r = 0;
    if ((threadIdx.x & 63) == 0) r = __hip_atomic_fetch_add((LAS int*)(sy + off), 1, __ATOMIC_RELAXED, __HIP_MEMORY_SCOPE_WORKGROUP);
    return RFL(r);
}
#define POLL_SLEEP 2
DI void flag_set(char* ws, unsigned off, u64 tag) {
    if ((threadIdx.x & 63) == 0) __hip_atomic_store((GAS u64*)(ws + off), tag, __ATOMIC_RELAXED, __HIP_MEMORY_SCOPE_AGENT);
}
template <int N>
DI void flag_wait(char* ws, unsigned laneoff, u64 tag) {
    GAS u64* p = (GAS u64*)(ws + laneoff);
    for (int spins = 0; spins < (1 << 20); ++spins) {
        bool ok = true;
#pragma unroll
        for (int i = 0; i < N; ++i) ok &= __hip_atomic_load(p + i * 64, __ATOMIC_RELAXED, __HIP_MEMORY_SCOPE_AGENT) == tag;
        if (__all(ok)) break;
        __builtin_amdgcn_s_sleep(POLL_SLEEP);
    }
    asm volatile("" ::: "memory");
}
#define WS_STORE(v, off) __builtin_amdgcn_raw_buffer_store_b128((v), wsr, (off), 0, 16)
#define WS_LOAD(voff, soff) __builtin_amdgcn_raw_buffer_load_b128(wsr, (voff), (soff), 16)

struct Geo {
    int lane, w, wm, h, l31, rot, q, tcol, sw;
};

template <int NB, bool SWAP, bool GATE, class Init, class Hook>
DI void proj_pass(lds_ptr lds, const Geo& g, int woff, f32x16 (&acc)[2], Init init, Hook hook) {
    const int xr = g.wm * 32 + g.l31;
    int xo[4];
#pragma unroll
    for (int s = 0; s < 4; ++s) xo[s] = L_RING + 16384 + xr * 128 + (((2 * s + g.h) ^ ((xr >> 1) & 7)) << 4);
    const int w0 = (L_RING + (8 * g.h + g.q) * 128 + g.sw + g.tcol) ^ woff, w1 = w0 ^ 64;
    constexpr int NF = NB + (GATE ? 1 : 0);
    bf16x8 xf[4], wf[4][NF];
    auto rd = [&](int s, int b, int t) {
        const int so = (t & 3) * 32768;
        if (b == 0) xf[s] = ldsr128(lds + xo[s] + so);
        if (b < NB) wf[s][b] = trd2(lds + ((b ? w1 : w0) + so) + s * 2048, 512);
        else wf[s][b] = ldsr128(lds + L_GATE + ((t + g.rot) & 7) * 128 + g.h * 16 + s * 32);
        SBAR();
    };
    auto mm = [&](int s, int b) {
        acc[b] = SWAP ? MFMA(xf[s], wf[s][b], acc[b]) : MFMA(wf[s][b], xf[s], acc[b]);
        SBAR();
    };
    LGKM_BAR(); SBAR();
    init();
#pragma unroll
    for (int s = 0; s < 4; ++s)
#pragma unroll
        for (int b = 0; b < NF; ++b) { if (s >= 2) mm(s - 2, b); rd(s, b, 0); }
#pragma unroll 1
    for (int t = 1; t < 8; ++t) {
        hook();
        LGKM_BAR(); SBAR();
#pragma unroll
        for (int s = 0; s < 4; ++s)
#pragma unroll
            for (int b = 0; b < NF; ++b) { mm((s + 2) & 3, b); rd(s, b, t); }
    }
#pragma unroll
    for (int s = 2; s < 4; ++s)
#pragma unroll
        for (int b = 0; b < NF; ++b) mm(s, b);
}
DI f32x16 bias16(lds_cptr p) {
    f32x16 v;
#pragma unroll
    for (int i = 0; i < 4; ++i) {
        const u32x2 bb = *(LAS const u32x2*)(p + 16 * i);
        v[4 * i] = bflo(bb[0]); v[4 * i + 1] = bfhi(bb[0]); v[4 * i + 2] = bflo(bb[1]); v[4 * i + 3] = bfhi(bb[1]);
    }
    return v;
}

#define LOOKA 8
#define PACE 3
#define PACE_LAG 0

__global__ __launch_bounds__(768, 3) void fused_block(const char* __restrict__ x, const char* __restrict__ wqkv, const char* __restrict__ wo,
                                                      const char* __restrict__ w1, const char* __restrict__ b1, const char* __restrict__ w2,
                                                      const char* __restrict__ gate, char* __restrict__ ws, char* __restrict__ out) {
    __shared__ __attribute__((aligned(1024))) char ldsmem[L_TOTAL];
    const int tid = threadIdx.x, lane = tid & 63, w = RFL(tid >> 6), wm = w & 3, h = lane >> 5, l31 = lane & 31;
    const int bid = blockIdx.x, xcd = bid & 7, idx = bid >> 3;
    const unsigned lds0 = (unsigned)(uintptr_t)ldsmem;
    const lds_ptr lds = (lds_ptr)ldsmem;
    const lds_ptr sy = lds + L_SYNC;
    STAMP_DECL STAMP_RT(); STAMP();
    u64 code_pc;
    unsigned code_len;
    asm volatile("s_getpc_b64 %0\n.Lcode_here:\n\ts_max_i32 %1, .Lcode_end_a-.Lcode_here, 0\n\ts_max_i32 %1, .Lcode_end_b-.Lcode_here, %1"
                 : "=s"(code_pc), "=&s"(code_len)::"scc");
    const int rb = (xcd & 3) * 4 + (idx & 3), c = (xcd >> 2) * 8 + (idx >> 2), j = c >> 1, odd = c & 1;
    const int rot = ((idx >> 2) + 2 * (idx & 3)) & 7;
    const int nh4 = ((j ^ c) & 1) == 0;
    const int hb0 = 7 * j + (odd ? (nh4 ? 3 : 4) : 0), hl0 = hb0 & ~1;
    const int head = xcd * 4 + (idx >> 3), qblk = idx & 7;
    const int mt = xcd * 4 + (idx >> 3), nt = idx & 7;
    u64 tag;
#define EPOCH_LOAD() asm volatile("s_load_dwordx2 %0, %1, 0x0" : "=&s"(tag) : "s"(ws + OFF_EP) : "memory")
#define EPOCH_WAIT() do { asm volatile("s_waitcnt lgkmcnt(0)" : "+s"(tag)::"memory"); tag += 1; } while (0)
    const __amdgpu_buffer_rsrc_t wsr = __builtin_amdgcn_make_buffer_rsrc(ws, 0, WS_USED, 0x00020000);
    const int wch = ((lane & 7) ^ (((lane >> 4) & 1) << 2)) * 16;
    const char* kg = ws + OFF_K + (size_t)head * 65536;
    const char* vg = ws + OFF_V + (size_t)head * 65536;

    if (__builtin_expect(w >= 8, 1)) {
        const int r8 = lane >> 3;
        const unsigned vx = (wm * 32 + r8) * 1024, cx0 = ((lane & 7) ^ (r8 >> 1)) << 4, cx1 = cx0 ^ 64;
        const unsigned vx0 = vx + cx0, vx1 = vx + 7168 + cx1, vx2 = vx + 2 * 7168 + cx0, vx3 = vx + 3 * 7168 + cx1;
        const unsigned vq = ((wm & 1) * 32 + r8) * 3072 + wch + (wm >> 1) * 1024, vh = ((wm & 1) * 32 + r8) * 3584 + wch + (wm >> 1) * 128;
        const char* xb = x + (size_t)rb * 128 * 1024;
        const char* qb = wqkv + j * 128 + odd * 1024;
        const char* hb = w1 + hl0 * 64;
        const unsigned rd0 = lds0 + L_RING + wm * 4096;
        auto unit = [&](int u) {
            const int ks = ((u >> 1) + rot) & 7;
            const unsigned d = rd0 + (u % NSLOT) * 16384;
            if (u & 1) glds4(xb + ks * 128, vx0, vx1, vx2, vx3, d);
            else if (u < 16) glds4(qb + ks * (64 * 3072), vq, vq + (8 * 3072 - 1024), vq + 2 * (8 * 3072 - 1024), vq + 3 * (8 * 3072 - 1024), d);
            else glds4(hb + ks * (64 * 3584), vh, vh + (8 * 3584 - 1024), vh + 2 * (8 * 3584 - 1024), vh + 3 * (8 * 3584 - 1024), d);
        };
        { const unsigned o1 = (lane & 11) == 0 ? 0x3f803f80u : 0u; *(LAS u32x4*)(lds + L_ONES + (tid - 512) * 16) = u32x4{o1, o1, o1, o1}; }
        if (w == 8) *(LAS int*)(sy + lane * 4) = 0;
        __builtin_amdgcn_s_setprio(1);
        unit(0); unit(1);
        EPOCH_LOAD();
        unit(2);
        EPOCH_WAIT();
        STAMP();
        VM_BAR(4);
        STAMP();
        unit(3); unit(4); unit(5); unit(6); unit(7);
        unsigned pf;
        u64 fq;
        {
            const int pt = bid < 224 ? bid : 223;
            const char* s2 = w2 + (size_t)(pt >> 3) * 65536 + (pt & 7) * 128;
            const unsigned v2 = (wm * 16 + r8) * 1024 + wch;
            glds16(s2, v2, lds0 + L_W2 + wm * 2048); glds16(s2 + 8192, v2, lds0 + L_W2 + wm * 2048 + 1024);
            if (wm & 1) glds1(b1 + hl0 * 64, lane * 4, lds0 + L_BIAS); else glds16(gate, lane * 16, lds0 + L_GATE);
            asm volatile("global_load_dword %0, %1, %2" : "=v"(pf) : "v"(((idx & 3) * 128 + wm * 32 + l31) * 3584 + h * 128), "s"(hb) : "memory");
        }
#pragma unroll
        for (int t = 1; t < 16; ++t) {
            if (t < 4) VM_BAR(20); else if (t < 14) VM_BAR(16); else if (t == 14) VM_BAR(8); else VM_BAR(0);
            if (2 * t + 6 < 32) { unit(2 * t + 6); unit(2 * t + 7); }
            if (t == 12) {
                asm volatile("global_load_dwordx2 %0, %1, %2 sc1" : "=v"(fq) : "v"(OFF_FQ + (head >> 2) * 256 + l31 * 8), "s"(ws) : "memory");
            }
            if (t == 4 || t == 8 || t == 12) STAMP();
        }
        PIN(pf); PIN(fq);
        STAMP();
        const bool early = __all(fq == tag);
        if (UNLIKELY(!early)) {
            asm volatile("s_barrier" ::: "memory");
            flag_wait<1>(ws, OFF_FQ + (head >> 2) * 256 + l31 * 8, tag);
        }
        const char* kvg = (wm & 1) ? vg : kg;
        const unsigned kvd = lds0 + L_KV + (wm & 1) * 32768 + (wm >> 1) * 4096;
        if (wm < 2) glds4c(ws + OFF_Q + (size_t)(head * SEQ + qblk * 256) * 32 + wm * 4096, lane * 16, lds0 + L_X + wm * 4096);
#pragma unroll
        for (int i = 0; i < 8; ++i) {
            const int hf = i < 2 ? i : (i < 5 ? 0 : 1), gi = i < 2 ? 0 : (i < 5 ? i - 1 : i - 4);
            if (i == 1 && early) asm volatile("s_barrier" ::: "memory");
            glds4c(kvg + hf * 32768 + (((wm >> 1) + 2 * gi + qblk) & 7) * 4096, lane * 16, kvd + hf * 65536 + gi * 8192);
            if (i == 1) { VMW(4); sig(sy, S_KVM + wm * 4, 1); }
            if (i == 2) { VMW(4); sig(sy, S_KVM + wm * 4, 2); }
            if (i == 5) { VMW(4); sig(sy, S_KVM + wm * 4, 3); }
            if (i == 3 || i == 4 || i == 6) VMW(4);
        }
        VMW(0); sig(sy, S_KVM + wm * 4, 4);
        STAMP();
        __builtin_amdgcn_s_setprio(3);
        if (w == 8) {
            flag_wait<1>(ws, h ? OFF_FH + ((mt >> 1) * 16 + (lane & 15)) * 8 : OFF_FW + (nt * 32 + (l31 < 28 ? l31 : 0)) * 8, tag);
            sig(sy, S_HOK, 1);
        } else spin1(sy, S_HOK, 1);
        STAMP();
        constexpr int PD = 4;
        const int hs = OFF_H + ((mt * 2 + (wm & 1)) * 112 + wm * 28) * 1024, hs1 = OFF_H + ((mt * 2 + ((wm & 1) ^ 1)) * 112 + wm * 28) * 1024;
        const int wso = OFF_W2F + ((nt * 2 + (wm >> 1)) * 112 + wm * 28) * 1024, wso1 = OFF_W2F + ((nt * 2 + ((wm >> 1) ^ 1)) * 112 + wm * 28) * 1024;
        u32x4 hf[PD][2], wf[PD][2];
        auto ld = [&](int i, int k) {
            hf[i][0] = WS_LOAD(lane * 16, hs + k * 1024); wf[i][0] = WS_LOAD(lane * 16, wso + k * 1024);
            hf[i][1] = WS_LOAD(lane * 16, hs1 + k * 1024); wf[i][1] = WS_LOAD(lane * 16, wso1 + k * 1024);
            SBAR();
        };
#pragma unroll
        for (int i = 0; i < PD; ++i) ld(i, i);
        f32x16 acc[2][2] = {};
        bool wopend = true;
        auto load_wo = [&] {
            const char* wg = wo + nt * 128 + (size_t)wm * 131072;
            const unsigned vow = r8 * 1024 + wch;
#pragma unroll
            for (int i = 0; i < 4; ++i) glds4(wg + i * 32768, vow, vow + 7168, vow + 2 * 7168, vow + 3 * 7168, lds0 + L_KV + wm * 32768 + i * 4096);
            wopend = false;
        };
        i32x4 pg0 = *(LAS volatile i32x4*)(sy + S_PROG), pg1 = *(LAS volatile i32x4*)(sy + S_PROG + 16);
        auto kstep = [&](int i, int k) {
            const int prog = RFL(min(min(min(pg0[0], pg0[1]), min(pg0[2], pg0[3])), min(min(pg1[0], pg1[1]), min(pg1[2], pg1[3]))));
            pg0 = *(LAS volatile i32x4*)(sy + S_PROG); pg1 = *(LAS volatile i32x4*)(sy + S_PROG + 16);
            if (UNLIKELY(wopend && prog >= 16)) load_wo();
            const int mode = prog >= 32 ? 0 : (k + PACE_LAG > prog ? 2 : 1);
#pragma unroll
            for (int a = 0; a < 4; ++a) {
                acc[a >> 1][a & 1] = MFMA(BC8(wf[i][a >> 1]), BC8(hf[i][a & 1]), acc[a >> 1][a & 1]);
                SBAR();
                asm volatile("s_cmp_eq_u32 %0, 0\n\ts_cbranch_scc1 1f\n\ts_sleep 1\n\ts_cmp_eq_u32 %0, 1\n\ts_cbranch_scc1 1f\n\ts_sleep %1\n1:" ::"s"(mode), "n"(PACE - 1) : "scc");
                SBAR();
            }
        };
#pragma unroll 1
        for (int k0 = 0; k0 < 28 - PD; k0 += PD) {
#pragma unroll
            for (int i = 0; i < PD; ++i) { kstep(i, k0 + i); ld(i, k0 + PD + i); }
        }
#pragma unroll
        for (int i = 0; i < PD; ++i) kstep(i, 28 - PD + i);
        STAMP();
        if (wopend) { spin8(sy, S_PROG, 16); load_wo(); }
        VMW(0);
        spin8(sy, S_PROG, 28);
#pragma unroll
        for (int r = 1; r < 4; ++r) {
            const f32x16& gv = acc[r >> 1][r & 1];
            LAS f32x4* dst = (LAS f32x4*)(lds + L_KV + wm * 32768 + 12288 + r * 4096) + lane;
#pragma unroll
            for (int i = 0; i < 4; ++i) dst[i * 64] = f32x4{gv[4 * i], gv[4 * i + 1], gv[4 * i + 2], gv[4 * i + 3]};
        }
        LGW(); sig(sy, S_EX + wm * 4, 1);
        spin4f(sy, S_EX, 1);
        f32x16 keep = acc[0][0];
#pragma unroll
        for (int r = 1; r < 4; ++r) {
            LAS const f32x4* src = (LAS const f32x4*)(lds + L_KV + (wm ^ r) * 32768 + 12288 + r * 4096) + lane;
#pragma unroll
            for (int i = 0; i < 4; ++i) {
                const f32x4 v = src[i * 64];
#pragma unroll
                for (int e = 0; e < 4; ++e) keep[4 * i + e] += v[e];
            }
        }
        { LAS u32x4* pb = (LAS u32x4*)(lds + L_W2 + wm * 2048) + lane; pb[0] = pk8(keep, 0); pb[64] = pk8(keep, 1); }
        LGW(); sig(sy, S_FB + wm * 4, 1);
        if (bid == 0 && w == 8) {
            flag_wait<4>(ws, OFF_FH + lane * 8, tag);
            flag_set(ws, OFF_EP, tag);
        }
        STAMP(); STAMP_RT(); STAMP_OUT(0, ws);
        asm volatile(".Lcode_end_a:");
        return;
    }

    Geo g;
    g.lane = lane; g.w = w; g.wm = wm; g.h = h; g.l31 = l31; g.rot = rot;
    g.q = (lane & 15) >> 2; g.tcol = ((lane >> 4) & 1) * 32 + (lane & 3) * 8; g.sw = (g.q >> 1) << 6;
    const int tok0 = rb * 128 + wm * 32, half = w >> 2;
    unsigned cpf = 0;
    if (w == 0) {
        const unsigned o = (lane * 32 + idx) * 128;
        if (o < code_len) asm volatile("global_load_dword %0, %1, %2" : "=v"(cpf) : "v"(o), "s"(code_pc) : "memory");
    }
    EPOCH_LOAD();
    EPOCH_WAIT();
    __builtin_amdgcn_s_setprio(2);
    {
        f32x16 acc[2] = {};
        auto nohook = [] {};
        const unsigned o = (4 * j * SEQ + tok0 + l31) * 32 + h * 16;
        if (half) {
            proj_pass<1, false, false>(lds, g, odd ? 64 : 8192, acc, nohook, nohook);
            STAMP();
#pragma unroll
            for (int s = 0; s < 2; ++s) WS_STORE(pk8(acc[0], s), OFF_K + o + (2 * odd + s) * SEQ * 32);
        } else if (!odd) {
            proj_pass<2, false, false>(lds, g, 0, acc, nohook, nohook);
            STAMP();
#pragma unroll
            for (int i = 0; i < 4; ++i) WS_STORE(pk8(acc[i >> 1] * QSCALE, i & 1), OFF_Q + o + i * SEQ * 32);
        } else {
            proj_pass<2, true, false>(lds, g, 8192, acc, nohook, nohook);
            STAMP();
            const unsigned ov = OFF_V + ((4 * j + (l31 >> 4)) * 128 + (tok0 >> 4)) * 512 + h * 256 + (lane & 15) * 16;
#pragma unroll
            for (int i = 0; i < 4; ++i) WS_STORE(pk8(acc[i >> 1], i & 1), ov + (i >> 1) * 2 * 65536 + (i & 1) * 512);
        }
    }
    WS_STORE((u32x4{POISON, POISON, POISON, POISON}), OFF_O + (((rb * 4 + wm) * 32 + 4 * j + 2 * odd + half) * 64 + lane) * 16);
    STAMP();
    {
        unsigned qpub = 0;
        auto pubq_now = [&] {
            VMW(0); PIN(cpf);
            if (lds_inc(sy, S_CNTQ) == 7) flag_set(ws, OFF_FQ + ((j * 16 + rb) * 2 + odd) * 8, tag);
            qpub = 1;
        };
        auto pubq = [&] { if (UNLIKELY((vm_cnt4() | qpub) == 0)) pubq_now(); };
        const int pB = (nh4 || (hb0 & 1)) ? 1 : 0;
        const int blk = half ? 2 * pB : (nh4 ? 0 : (pB ? 1 : 2));
        const int nb = (half || nh4) ? 2 : 1;
        f32x16 acc[2];
        const int woff = (blk >> 1) * 8192 + (blk & 1) * 64;
        auto init2 = [&] { acc[0] = bias16(lds + L_BIAS + blk * 64 + h * 8); acc[1] = bias16(lds + L_BIAS + blk * 64 + 64 + h * 8); };
        auto init1 = [&] { acc[0] = bias16(lds + L_BIAS + blk * 64 + h * 8); acc[1] = f32x16{}; };
        if (nb == 2) proj_pass<2, false, false>(lds, g, woff, acc, init2, pubq);
        else if (c == 1) proj_pass<1, false, true>(lds, g, woff, acc, init1, pubq);
        else proj_pass<1, false, false>(lds, g, woff, acc, init1, pubq);
        LGKM_BAR();
        __builtin_amdgcn_s_setprio(3);
        if (!qpub) pubq_now();
        STAMP();
        const unsigned oh = OFF_H + (((tok0 >> 5) * 112 + (hl0 + blk) * 2) * 64 + lane) * 16;
        { const f32x16 v = mish16(acc[0]); WS_STORE(pk8(v, 0), oh); WS_STORE(pk8(v, 1), oh + 1024); }
        if (nb == 2) { const f32x16 v = mish16(acc[1]); WS_STORE(pk8(v, 0), oh + 2048); WS_STORE(pk8(v, 1), oh + 3072); }
        else if (c == 1) {
            const float e = __builtin_amdgcn_exp2f(acc[1][0] * -LOG2E);
            if (h == 0) __hip_atomic_store((GAS float*)(ws + OFF_G) + tok0 + l31, __builtin_amdgcn_rcpf(1.0f + e), __ATOMIC_RELAXED, __HIP_MEMORY_SCOPE_AGENT);
        }
        if (bid < 224) {
            const bf16x8 f = trd2(lds + L_W2 + (16 * wm + 4 * h + g.q) * 128 + g.tcol + (g.sw ^ (half * 64)), 1024);
            WS_STORE(__builtin_bit_cast(u32x4, f), OFF_W2F + ((((bid & 7) * 2 + half) * 112 + (bid >> 3) * 4 + wm) * 64 + lane) * 16);
        }
    }
    STAMP();
    unsigned hpub = 0;
    auto pubh_now = [&] {
        VMW(0);
        if (lds_inc(sy, S_CNTH) == 7) {
            flag_set(ws, OFF_FH + (rb * 16 + c) * 8, tag);
            if (bid < 224) flag_set(ws, OFF_FW + ((bid & 7) * 32 + (bid >> 3)) * 8, tag);
        }
        hpub = 1;
        __builtin_amdgcn_s_setprio(2);
    };
    auto pubh = [&] { if (UNLIKELY((vm_cnt4() | hpub) == 0)) pubh_now(); };
    while (min4(sy, S_KVM) < 1 + half) { pubh(); __builtin_amdgcn_s_sleep(1); }
    asm volatile("" ::: "memory");
    STAMP();
    const bf16x8 qA = ldsr128(lds + L_X + wm * 2048 + l31 * 32 + h * 16), qB = ldsr128(lds + L_X + wm * 2048 + 1024 + l31 * 32 + h * 16);
    lds_cptr kp = lds + L_KV + half * 65536 + l31 * 32 + h * 16;
    const bool isone = (lane & 16) != 0;
    lds_cptr vp = lds + (isone ? L_ONES : L_KV + half * 65536 + 32768) + h * 256 + (lane & 15) * 16;
    const int vstep = isone ? 0 : 4096;
    f32x16 OA = {}, OB = {}, SA, SB;
    const f32x16 Z = {};
    bf16x8 kf = ldsr128(kp), v0 = ldsr128(vp), v1 = ldsr128(vp + 512);
    u32x4 p0, p1 = {0, 0, 0, 0};
    SA = MFMA(kf, qA, Z);
#define EX(i) S_[i] = __builtin_amdgcn_exp2f(S_[i])
#define UNIT(S, SN, QN, O, OP, LD0, LD1, LD2)                                                       \
    {                                                                                               \
        f32x16& S_ = S;                                                                             \
        SN = MFMA(kf, QN, Z); PIN(SN); SBAR();                                                      \
        LD0; EX(0); EX(1); PIN(S_); SBAR();                                                         \
        OP = MFMA(v1, BC8(p1), OP); PIN(OP); SBAR();                                                \
        LD1; EX(2); EX(3); EX(4); EX(5); EX(6); EX(7); PIN(S_); SBAR();                             \
        p0 = pk8(S_, 0); PIN(p0); SBAR();                                                           \
        EX(8); EX(9); PIN(S_); SBAR();                                                              \
        O = MFMA(v0, BC8(p0), O); PIN(O); SBAR();                                                   \
        LD2; EX(10); EX(11); EX(12); EX(13); EX(14); EX(15); PIN(S_); SBAR();                       \
        p1 = pk8(S_, 1); PIN(p1); SBAR();                                                           \
    }
#define TILE(t, u)                                                                                  \
    UNIT(SA, SB, qB, OA, OB, kf = ldsr128(kp + ((u) + 1) * 1024), v1 = ldsr128(vp + (u) * 1024 + 512), (void)0) \
    UNIT(SB, SA, qA, OB, OA, (void)0, (void)0, v0 = ldsr128((u) == 3 ? vp + vstep : vp + ((u) + 1) * 1024))
    int t = 0;
#pragma unroll 1
    for (int ph = 0; ph < 3; ++ph) {
        if (ph == 1) __builtin_amdgcn_s_setprio(1);
        if (ph == 2) { __builtin_amdgcn_s_setprio(0); spin8(sy, S_PROG, 4); }
        const int tend = ph == 2 ? 32 : 12 + 12 * ph;
#pragma unroll 1
        do {
            sig(sy, S_PROG + w * 4, t);
            const i32x4 fl = *(LAS volatile i32x4*)(sy + S_KVM);
            TILE(t, 0) pubh(); TILE(t + 1, 1) pubh(); TILE(t + 2, 2) pubh();
            if (UNLIKELY(RFL(min(min(fl[0], fl[1]), min(fl[2], fl[3]))) < (t ? 3 + half : 0))) spin4(sy, S_KVM, 3 + half);
            TILE(t + 3, 3) pubh();
            vp += vstep; kp += 4096;
            t += 4;
        } while (t < tend);
    }
    if (!hpub) pubh_now();
    OB = MFMA(v1, BC8(p1), OB);
    PIN(kf); PIN(v0);
    LGW();
    sig(sy, S_PROG + w * 4, 32);
    STAMP();
    {
        const f32x16 give = half ? OA : OB;
        LAS f32x4* dst = (LAS f32x4*)(lds + L_X + w * 2048) + lane;
        dst[0] = f32x4{give[0], give[1], give[2], give[3]};
        dst[64] = f32x4{give[4], give[5], give[6], give[7]};
        ((LAS float*)(lds + L_GATE + w * 128))[l31] = give[8];
        LGW(); sig(sy, S_FX + w * 4, 1);
    }
    f32x16 O = half ? OB : OA;
    spin1(sy, S_FX + (w ^ 4) * 4, 1);
    {
        LAS const f32x4* src = (LAS const f32x4*)(lds + L_X + (w ^ 4) * 2048) + lane;
        const f32x4 a = src[0], b = src[64];
#pragma unroll
        for (int r = 0; r < 4; ++r) { O[r] += a[r]; O[4 + r] += b[r]; }
        O[8] += ((LAS const float*)(lds + L_GATE + (w ^ 4) * 128))[l31];
    }
    float asum = __builtin_fabsf(O[0]);
#pragma unroll
    for (int r = 1; r < 8; ++r) asum += __builtin_fabsf(O[r]);
    const bool bad = !(O[8] > 0x1p-90f && O[8] < 0x1p100f && asum < 0x1p120f);
    if (UNLIKELY(__any(bad))) {
        const bf16x8 qf = half ? qB : qA;
        const unsigned o1 = (lane & 11) == 0 ? 0x3f803f80u : 0u;
        float m = -INFINITY;
        O = Z;
#pragma unroll 1
        for (int t = 0; t < 64; ++t) {
            f32x16 s = MFMA(BC8(WS_LOAD(l31 * 32 + h * 16, OFF_K + head * 65536 + t * 1024)), qf, Z);
            u32x4 va = WS_LOAD(h * 256 + (lane & 15) * 16, OFF_V + head * 65536 + t * 1024);
            u32x4 vb = WS_LOAD(h * 256 + (lane & 15) * 16, OFF_V + head * 65536 + t * 1024 + 512);
            if (isone) { va = u32x4{o1, o1, o1, o1}; vb = va; }
            float tm = s[0];
#pragma unroll
            for (int r = 1; r < 16; ++r) tm = __builtin_fmaxf(tm, s[r]);
            tm = __builtin_fmaxf(tm, __shfl_xor(tm, 32));
            const float mn = __builtin_fmaxf(m, tm);
            const float al = __builtin_amdgcn_exp2f(m - mn);
            m = mn;
#pragma unroll
            for (int r = 0; r < 16; ++r) { O[r] *= al; s[r] = __builtin_amdgcn_exp2f(s[r] - mn); }
            O = MFMA(BC8(va), BC8(pk8(s, 0)), O);
            O = MFMA(BC8(vb), BC8(pk8(s, 1)), O);
        }
    }
    {
        u32x4 ov = pk8(O * __builtin_amdgcn_rcpf(O[8]), 0);
#pragma unroll
        for (int i = 0; i < 4; ++i) ov[i] = ov[i] == POISON ? 0x7fff7fffu : ov[i];
        WS_STORE(ov, OFF_O + (((qblk * 8 + wm * 2 + half) * 32 + head) * 64 + lane) * 16);
    }
    sig(sy, S_FM + w * 4, 1);
    STAMP();
    const int wn = (w >> 1) & 1, wmm = w & 1;
    const int row = mt * 64 + wmm * 32 + l31;
    float gv = 0.f;
    spin4(sy, S_FB, 1);
    spin8(sy, S_PROG, 32);
    if (w < 4) gv = __hip_atomic_load((GAS float*)(ws + OFF_G) + row, __ATOMIC_RELAXED, __HIP_MEMORY_SCOPE_AGENT);
    const char* og = ws + OFF_O + (size_t)mt * 65536;
    {
        const int kq = ((w >> 1) + nt) & 3;
        const char* src = og + wmm * 32768 + kq * 8192;
        const unsigned dst = lds0 + L_KV + 16384 + kq * 32768 + wmm * 8192;
        glds4c(src, lane * 16, dst); glds4c(src + 4096, lane * 16, dst + 4096);
    }
    STAMP();
    f32x16 bq;
    {
        LAS const u32x4* pb = (LAS const u32x4*)(lds + L_W2 + wm * 2048) + lane;
        const u32x4 b0 = pb[0], b1v = pb[64];
#pragma unroll
        for (int i = 0; i < 4; ++i) { bq[2 * i] = bflo(b0[i]); bq[2 * i + 1] = bfhi(b0[i]); bq[8 + 2 * i] = bflo(b1v[i]); bq[8 + 2 * i + 1] = bfhi(b1v[i]); }
    }
    spin8(sy, S_FM, 1);
    VMW(0); sig(sy, S_OF + w * 4, 1);
    bq *= 1.0f - gv;
    while (min8(sy, S_OF) < 1) {}
    asm volatile("" ::: "memory");
    STAMP();
    const lds_cptr wa = lds + L_KV + half * 65536 + (4 * h + g.q) * 128 + (g.sw ^ (wn * 64)) + g.tcol;
    const lds_cptr oa = lds + L_KV + 16384 + half * 65536 + wmm * 8192 + lane * 16;
    f32x16 ag[4];
    unsigned badg = 0;
#pragma unroll
    for (int q4 = 0; q4 < 4; ++q4) {
        unsigned mx = 0;
        ag[q4] = f32x16{};
#pragma unroll
        for (int i = 0; i < 4; ++i) {
            const u32x4 of = *(LAS const u32x4*)(oa + (q4 >> 1) * 32768 + ((q4 & 1) * 4 + i) * 1024);
            ag[q4] = MFMA(trd2(wa + (q4 >> 1) * 32768 + ((q4 & 1) * 4 + i) * 2048, 1024), BC8(of), ag[q4]);
            mx = max(max(mx, max(of[0], of[1])), max(of[2], of[3]));
        }
        badg |= __any(mx == POISON) ? 1u << q4 : 0u;
    }
    if (UNLIKELY(badg)) {
        const int oso = OFF_O + ((mt * 2 + wmm) * 32 + half * 16) * 1024;
        int budget = 1 << 18;
#pragma unroll
        for (int q4 = 0; q4 < 4; ++q4)
            if ((badg >> q4) & 1) {
                u32x4 of[4];
                bf16x8 wv[4];
#pragma unroll
                for (int i = 0; i < 4; ++i) wv[i] = trd2(wa + (q4 >> 1) * 32768 + ((q4 & 1) * 4 + i) * 2048, 1024);
                for (;;) {
                    unsigned mx = 0;
#pragma unroll
                    for (int i = 0; i < 4; ++i) of[i] = WS_LOAD(lane * 16, oso + (q4 * 4 + i) * 1024);
#pragma unroll
                    for (int i = 0; i < 4; ++i) mx = max(max(mx, max(of[i][0], of[i][1])), max(of[i][2], of[i][3]));
                    if (!__any(mx == POISON) || --budget < 0) break;
                    __builtin_amdgcn_s_sleep(POLL_SLEEP);
                }
                ag[q4] = f32x16{};
#pragma unroll
                for (int i = 0; i < 4; ++i) ag[q4] = MFMA(wv[i], BC8(of[i]), ag[q4]);
            }
    }
    f32x16 acc = (ag[0] + ag[1]) + (ag[2] + ag[3]);
    STAMP();
    LAS f32x4* ex = (LAS f32x4*)(lds + L_X + wm * 4096) + lane;
    if (w >= 4) {
#pragma unroll
        for (int i = 0; i < 4; ++i) ex[i * 64] = f32x4{acc[4 * i], acc[4 * i + 1], acc[4 * i + 2], acc[4 * i + 3]};
        LGW(); sig(sy, S_FY + wm * 4, 1);
    } else {
        while (RFL(*(LAS volatile int*)(sy + S_FY + wm * 4)) < 1) {}
        asm volatile("" ::: "memory");
#pragma unroll
        for (int i = 0; i < 4; ++i) {
            const f32x4 r = ex[i * 64];
#pragma unroll
            for (int e = 0; e < 4; ++e) acc[4 * i + e] += r[e];
        }
        const f32x16 res = acc * gv + bq;
        const u32x4 lo = pk8(res, 0), hi = pk8(res, 1);
#pragma unroll
        for (int k = 0; k < 2; ++k) {
            const u32x4 o = k ? hi : lo;
            auto r0 = __builtin_amdgcn_permlane32_swap(o[0], o[2], false, false);
            auto r1 = __builtin_amdgcn_permlane32_swap(o[1], o[3], false, false);
            *(GAS u32x4*)(out + (size_t)row * 1024 + (nt * 64 + wn * 32 + 16 * k + 8 * h) * 2) = u32x4{r0[0], r1[0], r0[1], r1[1]};
        }
    }
    STAMP(); STAMP_RT(); STAMP_OUT(0, ws);
    asm volatile(".Lcode_end_b:");
}

extern "C" void kernel_launch(void* const* d_in, const int* in_sizes, int n_in, void* d_out, int out_size, void* d_ws, size_t ws_size,
                              hipStream_t stream) {
    fused_block<<<256, 768, 0, stream>>>((const char*)d_in[0], (const char*)d_in[1], (const char*)d_in[2], (const char*)d_in[4],
                                         (const char*)d_in[5], (const char*)d_in[6], (const char*)d_in[7], (char*)d_ws, (char*)d_out);
}
```
